```python
import torch

def _softmax(x, axis=-1):
    return torch.softmax(x, dim=axis)

def _gelu(x):
    return torch.nn.functional.gelu(x, approximate='tanh')

def _silu(x):
    return torch.nn.functional.silu(x)

def _sigmoid(x):
    return torch.sigmoid(x)

def _layernorm(x, axis=-1, eps=1e-05):
    m = x.mean(dim=axis, keepdim=True)
    v = x.var(dim=axis, keepdim=True, unbiased=False)
    return (x - m) / torch.sqrt(v + eps)

def _scatter_add(tgt, idx, src, axis=0):
    return tgt.clone().index_add_(axis, idx, src)

def _segment_sum(src, seg, n):
    out = torch.zeros((n,) + src.shape[1:], dtype=src.dtype)
    return out.index_add_(0, seg, src)

def _pad(x, pw):
    flat = tuple((v for pair in reversed(pw) for v in pair))
    return torch.nn.functional.pad(x, flat)
kernel_name = 'adaLN_cond__gqa_block_cached__geglu_ffn__decode'

def setup_inputs():

    _w = lambda a: (lambda t: t.to(torch.bfloat16) if t.is_floating_point() else t)(torch.as_tensor(a))

    def _A_setup():
        B, Sq, D = (1, 1, 1024)
        Hq, Hkv, Sk, Dh = (32, 4, 65536, 128)
        assert Hq % Hkv == 0, (Hq, Hkv)
        return {'x': _w((lambda *s: torch.randn(*s, dtype=torch.bfloat16) * 0.05)(B, Sq, D)), 'k_cache': _w((lambda *s: torch.randn(*s, dtype=torch.bfloat16) * 3.2)(B, Hkv, Sk, Dh)), 'v_cache': _w((lambda *s: torch.randn(*s, dtype=torch.bfloat16) * 0.4)(B, Hkv, Sk, Dh)), 'wq': _w((lambda *s: torch.randn(*s, dtype=torch.bfloat16) * 3.2)(D, Hq * Dh) * D ** (-0.5)), 'wo': _w((lambda *s: torch.randn(*s, dtype=torch.bfloat16) * 0.4)(Hq * Dh, D) * (Hq * Dh) ** (-0.5))}
    _A_in = _A_setup()

    def _B_setup():
        B, S, D, F = (1, 1, 1024, 3584)
        return {'x': _w((lambda *s: torch.randn(*s, dtype=torch.bfloat16) * 0.05)(B, S, D)), 'w_gate': _w((lambda *s: torch.randn(*s, dtype=torch.bfloat16) * 0.05)(D, F)), 'w_up': _w((lambda *s: torch.randn(*s, dtype=torch.bfloat16) * 0.05)(D, F)), 'w_down': _w((lambda *s: torch.randn(*s, dtype=torch.bfloat16) * 0.05)(F, D))}
    _B_in = _B_setup()
    return {'A_x': _A_in['x'], 'A_k_cache': _A_in['k_cache'], 'A_v_cache': _A_in['v_cache'], 'A_wq': _A_in['wq'], 'A_wo': _A_in['wo'], 'B_x': _B_in['x'], 'B_w_gate': _B_in['w_gate'], 'B_w_up': _B_in['w_up'], 'B_w_down': _B_in['w_down'], 'c': _w((lambda *s: torch.randn(*s, dtype=torch.bfloat16) * 0.4)(1, 1024) * 8.0), 'w_mod': _w((lambda *s: torch.randn(*s, dtype=torch.bfloat16) * 0.4)(1024, 6 * 1024) * 1024 ** (-0.5) * 8.0)}

def reference(A_x, A_k_cache, A_v_cache, A_wq, A_wo, B_x, B_w_gate, B_w_up, B_w_down, c, w_mod):

    def _A_ref(x, k_cache, v_cache, wq, wo):
        B, Sq, D = x.shape
        Hq, Dh = (32, 128)
        Hkv = k_cache.shape[1]
        rep = Hq // Hkv
        q = (lambda x, *d: x.permute(*d))((x @ wq).reshape(B, Sq, Hq, Dh), 0, 2, 1, 3)
        k = (lambda x, n, axis: torch.repeat_interleave(x, n, dim=axis))(k_cache, rep, axis=1)
        v = (lambda x, n, axis: torch.repeat_interleave(x, n, dim=axis))(v_cache, rep, axis=1)
        scores = torch.einsum('bhqd,bhkd->bhqk', q, k) * Dh ** (-0.5)
        p = _softmax(scores, axis=-1)
        o = torch.einsum('bhqk,bhkd->bhqd', p, v)
        return (lambda x, *d: x.permute(*d))(o, 0, 2, 1, 3).reshape(B, Sq, Hq * Dh) @ wo

    def _B_ref(x, w_gate, w_up, w_down):
        return _gelu(x @ w_gate) * (x @ w_up) @ w_down
    x = A_x
    mod = (_silu(c) @ w_mod).reshape(c.shape[0], 6, 1, -1)
    sa, ca, ga, sb, cb, gb = (mod[:, 0].clone(), mod[:, 1].clone(), mod[:, 2].clone(), mod[:, 3].clone(), mod[:, 4].clone(), mod[:, 5].clone())
    h = _layernorm(x, axis=-1) * (1 + ca) + sa
    x = x + ga * _A_ref(h, A_k_cache, A_v_cache, A_wq, A_wo)
    h = _layernorm(x, axis=-1) * (1 + cb) + sb
    x = x + gb * _B_ref(h, B_w_gate, B_w_up, B_w_down)
    return x
```

```cpp
#include <hip/hip_runtime.h>
#include <hip/hip_bf16.h>
#include <stdint.h>

typedef unsigned short u16;
using u32x4 = __attribute__((ext_vector_type(4))) unsigned;
using f32x4 = __attribute__((ext_vector_type(4))) float;
using bf16x8 = __attribute__((ext_vector_type(8))) __bf16;

constexpr int D = 1024, HQ = 32, HKV = 4, SK = 65536, DH = 128, FF = 3584;
constexpr int NSPLIT = 64;
constexpr int PREC = 132;

constexpr size_t WS_CNT = 0;
constexpr size_t WS_MOD = 4096;
constexpr size_t WS_Q = WS_MOD + 6144 * 4;
constexpr size_t WS_X1 = WS_Q + 4096 * 4;
constexpr size_t WS_ACT = WS_X1 + 1024 * 4;
constexpr size_t WS_WOP = WS_ACT + 3584 * 4;
constexpr size_t WS_DNP = WS_WOP + 32 * 1024 * 4;
constexpr size_t WS_PART = WS_DNP + 4 * 1024 * 4;

__device__ __forceinline__ float bf2f(u16 v) { return __uint_as_float(((unsigned)v) << 16); }
__device__ __forceinline__ float bfr(float x) { return (float)(__bf16)x; }
__device__ __forceinline__ float lo16(unsigned w) { return __uint_as_float(w << 16); }
__device__ __forceinline__ float hi16(unsigned w) { return __uint_as_float(w & 0xffff0000u); }
__device__ __forceinline__ unsigned cvtpk(float lo, float hi) {
  typedef __attribute__((ext_vector_type(2))) __bf16 bf16x2_t;
  typedef __attribute__((ext_vector_type(2))) float f32x2_t;
  const f32x2_t v = {lo, hi};
  return __builtin_bit_cast(unsigned, __builtin_convertvector(v, bf16x2_t));
}
__device__ __forceinline__ u32x4 ldnt(const void* p) { return __builtin_nontemporal_load(reinterpret_cast<const u32x4*>(p)); }
#define PIN() do { asm volatile("" ::: "memory"); __builtin_amdgcn_sched_barrier(0); } while (0)
__device__ __forceinline__ void st_ag(float* p, float v) {
  __hip_atomic_store(reinterpret_cast<unsigned*>(p), __float_as_uint(v), __ATOMIC_RELAXED, __HIP_MEMORY_SCOPE_AGENT);
}
__device__ __forceinline__ float ld_ag(const float* p) {
  return __uint_as_float(__hip_atomic_load(reinterpret_cast<const unsigned*>(p), __ATOMIC_RELAXED, __HIP_MEMORY_SCOPE_AGENT));
}
__device__ __forceinline__ float xrow16_max(float x) {
  auto s = __builtin_amdgcn_permlane16_swap(__float_as_uint(x), __float_as_uint(x), false, false);
  x = fmaxf(__uint_as_float(s[0]), __uint_as_float(s[1]));
  auto t = __builtin_amdgcn_permlane32_swap(__float_as_uint(x), __float_as_uint(x), false, false);
  return fmaxf(__uint_as_float(t[0]), __uint_as_float(t[1]));
}
__device__ __forceinline__ float xrow16_sum(float x) {
  auto s = __builtin_amdgcn_permlane16_swap(__float_as_uint(x), __float_as_uint(x), false, false);
  x = __uint_as_float(s[0]) + __uint_as_float(s[1]);
  auto t = __builtin_amdgcn_permlane32_swap(__float_as_uint(x), __float_as_uint(x), false, false);
  return __uint_as_float(t[0]) + __uint_as_float(t[1]);
}
__device__ __forceinline__ void fma8(float (&acc)[8], float xv, u32x4 w) {
#pragma unroll
  for (int k = 0; k < 4; ++k) { acc[2 * k] = fmaf(xv, lo16(w[k]), acc[2 * k]); acc[2 * k + 1] = fmaf(xv, hi16(w[k]), acc[2 * k + 1]); }
}

__device__ __forceinline__ float block_sum256(float v, float* red) {
#pragma unroll
  for (int o = 32; o >= 1; o >>= 1) v += __shfl_xor(v, o);
  __syncthreads();
  if ((threadIdx.x & 63) == 0) red[threadIdx.x >> 6] = v;
  __syncthreads();
  return (red[0] + red[1]) + (red[2] + red[3]);
}

__device__ __forceinline__ void ln_mod(const float* xs, const float* shift, const float* scale, float* hs, float* red) {
  const int t = threadIdx.x;
  float xv[4], s = 0.f;
#pragma unroll
  for (int i = 0; i < 4; ++i) { xv[i] = xs[t + 256 * i]; s += xv[i]; }
  const float mean = block_sum256(s, red) * (1.f / 1024.f);
  float q = 0.f;
#pragma unroll
  for (int i = 0; i < 4; ++i) { const float d = xv[i] - mean; q = fmaf(d, d, q); }
  const float var = block_sum256(q, red) * (1.f / 1024.f);
  const float mb = bfr(mean), vb = bfr(var);
  const float sd = bfr(sqrtf(bfr(vb + 1e-5f)));
#pragma unroll
  for (int i = 0; i < 4; ++i) {
    const int e = t + 256 * i;
    const float ln = bfr(bfr(xv[i] - mb) / sd);
    const float tt = bfr(1.f + scale[e]);
    hs[e] = bfr(bfr(ln * tt) + shift[e]);
  }
  __syncthreads();
}

__device__ __forceinline__ float reduce16(float (&acc)[8], float* red  ) {
  const int lane = threadIdx.x & 63, wave = threadIdx.x >> 6;
#pragma unroll
  for (int o = 2; o <= 32; o <<= 1)
#pragma unroll
    for (int j = 0; j < 8; ++j) acc[j] += __shfl_xor(acc[j], o);
  __syncthreads();
  if (lane < 2) {
#pragma unroll
    for (int j = 0; j < 8; ++j) red[wave * 16 + lane * 8 + j] = acc[j];
  }
  __syncthreads();
  float r = 0.f;
  if (threadIdx.x < 16) r = (red[threadIdx.x] + red[16 + threadIdx.x]) + (red[32 + threadIdx.x] + red[48 + threadIdx.x]);
  return r;
}

__global__ void __launch_bounds__(256) k_mod(const u16* __restrict__ c, const u16* __restrict__ wmod, unsigned char* ws) {
  __shared__ float red[64];
  const int b = blockIdx.x, cg = (b & 7) * 48 + (b >> 3);
  const int lane = threadIdx.x & 63, wave = threadIdx.x >> 6;
  if (b == 0 && threadIdx.x < 128) reinterpret_cast<unsigned*>(ws + WS_CNT)[threadIdx.x] = 0u;
  const int rin = lane >> 1, half = lane & 1;
  u32x4 w[8];
#pragma unroll
  for (int i = 0; i < 8; ++i) w[i] = ldnt(wmod + (size_t)(wave * 256 + i * 32 + rin) * 6144 + cg * 16 + half * 8);
  PIN();
  float acc[8] = {0, 0, 0, 0, 0, 0, 0, 0};
#pragma unroll
  for (int i = 0; i < 8; ++i) {
    const float cv = bf2f(c[wave * 256 + i * 32 + rin]);
    const float sv = bfr(cv / (1.f + __expf(-cv)));
    fma8(acc, sv, w[i]);
  }
  const float r = reduce16(acc, red);
  if (threadIdx.x < 16) reinterpret_cast<float*>(ws + WS_MOD)[cg * 16 + threadIdx.x] = bfr(r);
}

__global__ void __launch_bounds__(256) k_q(const u16* __restrict__ x, const u16* __restrict__ wq, unsigned char* ws) {
  __shared__ float xs[1024], hs[1024], red[64];
  const int b = blockIdx.x, cg = (b & 7) * 32 + (b >> 3);
  const int lane = threadIdx.x & 63, wave = threadIdx.x >> 6;
  const int rin = lane >> 1, half = lane & 1;
  u32x4 w[8];
#pragma unroll
  for (int i = 0; i < 8; ++i) w[i] = ldnt(wq + (size_t)(wave * 256 + i * 32 + rin) * 4096 + cg * 16 + half * 8);
  PIN();
#pragma unroll
  for (int i = 0; i < 4; ++i) xs[threadIdx.x + 256 * i] = bf2f(x[threadIdx.x + 256 * i]);
  __syncthreads();
  const float* mod = reinterpret_cast<const float*>(ws + WS_MOD);
  ln_mod(xs, mod, mod + 1024, hs, red);
  float acc[8] = {0, 0, 0, 0, 0, 0, 0, 0};
#pragma unroll
  for (int i = 0; i < 8; ++i) fma8(acc, hs[wave * 256 + i * 32 + rin], w[i]);
  const float r = reduce16(acc, red);
  if (threadIdx.x < 16) reinterpret_cast<float*>(ws + WS_Q)[cg * 16 + threadIdx.x] = bfr(r);
}

constexpr int AW = 8;
constexpr int KPW = SK / NSPLIT / AW;
constexpr int NSTEP = KPW / 32;
__global__ void __launch_bounds__(512, 2) k_attn(const u16* __restrict__ kc, const u16* __restrict__ vc, unsigned char* ws) {
  __shared__ float mrg[AW * 8 * PREC];
  const int b = blockIdx.x, kvh = b >> 6, sp = b & 63;
  const int lane = threadIdx.x & 63, wave = __builtin_amdgcn_readfirstlane(threadIdx.x >> 6);
  const int r = lane & 15, g = lane >> 4;
  const size_t base = ((size_t)kvh * SK + (size_t)sp * (SK / NSPLIT) + wave * KPW + r) * DH + g * 8;
  const u16* kp = kc + base;
  const u16* vp = vc + base;
  u32x4 kb[2][8], vb[2][8];
#define LOADSTEP(buf, step)                                                                  \
  _Pragma("unroll") for (int t_ = 0; t_ < 2; ++t_) _Pragma("unroll") for (int s_ = 0; s_ < 4; ++s_) { \
    kb[buf][t_ * 4 + s_] = ldnt(kp + ((step) * 32 + t_ * 16) * DH + s_ * 32);                  \
    vb[buf][t_ * 4 + s_] = ldnt(vp + ((step) * 32 + t_ * 16) * DH + s_ * 32); }
  LOADSTEP(0, 0)
  PIN();
  u32x4 qf[4];
  {
    const float* q = reinterpret_cast<const float*>(ws + WS_Q) + (kvh * 8 + (r & 7)) * DH + g * 8;
#pragma unroll
    for (int s = 0; s < 4; ++s) {
      const f32x4 a = *reinterpret_cast<const f32x4*>(q + s * 32), c = *reinterpret_cast<const f32x4*>(q + s * 32 + 4);
      u32x4 v = {cvtpk(a[0], a[1]), cvtpk(a[2], a[3]), cvtpk(c[0], c[1]), cvtpk(c[2], c[3])};
      if (r >= 8) v = u32x4{0u, 0u, 0u, 0u};
      qf[s] = v;
    }
  }
  u32x4 sel[2];
#pragma unroll
  for (int h = 0; h < 2; ++h) {
    const int kk = r + 16 * h - 8 * g;
#pragma unroll
    for (int w = 0; w < 4; ++w) sel[h][w] = (kk == 2 * w ? 0x3f80u : 0u) | (kk == 2 * w + 1 ? 0x3f800000u : 0u);
  }
  f32x4 acc[8];
#pragma unroll
  for (int i = 0; i < 8; ++i) acc[i] = f32x4{0.f, 0.f, 0.f, 0.f};
  float m = -INFINITY, l = 0.f;
  constexpr float SCALE = 0.08838834764831845f, LOG2E = 1.4426950408889634f;
#pragma unroll
  for (int st = 0; st < NSTEP; ++st) {
    const int cur = st & 1;
    if (st + 1 < NSTEP) { LOADSTEP(cur ^ 1, st + 1) }
    PIN();
    f32x4 s0 = {0.f, 0.f, 0.f, 0.f}, s1 = {0.f, 0.f, 0.f, 0.f};
#pragma unroll
    for (int s = 0; s < 4; ++s) {
      s0 = __builtin_amdgcn_mfma_f32_16x16x32_bf16(__builtin_bit_cast(bf16x8, kb[cur][s]), __builtin_bit_cast(bf16x8, qf[s]), s0, 0, 0, 0);
      s1 = __builtin_amdgcn_mfma_f32_16x16x32_bf16(__builtin_bit_cast(bf16x8, kb[cur][4 + s]), __builtin_bit_cast(bf16x8, qf[s]), s1, 0, 0, 0);
    }
    float t[8];
#pragma unroll
    for (int j = 0; j < 4; ++j) { t[j] = bfr(bfr(s0[j]) * SCALE); t[4 + j] = bfr(bfr(s1[j]) * SCALE); }
    float mx = t[0];
#pragma unroll
    for (int j = 1; j < 8; ++j) mx = fmaxf(mx, t[j]);
    mx = xrow16_max(mx);
    const float mn = fmaxf(m, mx);
    const float alpha = __builtin_amdgcn_exp2f((m - mn) * LOG2E);
    m = mn;
    float p[8], ps = 0.f;
#pragma unroll
    for (int j = 0; j < 8; ++j) { p[j] = __builtin_amdgcn_exp2f((t[j] - mn) * LOG2E); ps += p[j]; }
    l = fmaf(l, alpha, ps);
    const u32x4 pb = {cvtpk(p[0], p[1]), cvtpk(p[2], p[3]), cvtpk(p[4], p[5]), cvtpk(p[6], p[7])};
#pragma unroll
    for (int i = 0; i < 8; ++i) acc[i] *= alpha;
#pragma unroll
    for (int db = 0; db < 8; ++db) {
      const f32x4 z = {0.f, 0.f, 0.f, 0.f};
      const f32x4 d0 = __builtin_amdgcn_mfma_f32_16x16x32_bf16(__builtin_bit_cast(bf16x8, vb[cur][db >> 1]), __builtin_bit_cast(bf16x8, sel[db & 1]), z, 0, 0, 0);
      const f32x4 d1 = __builtin_amdgcn_mfma_f32_16x16x32_bf16(__builtin_bit_cast(bf16x8, vb[cur][4 + (db >> 1)]), __builtin_bit_cast(bf16x8, sel[db & 1]), z, 0, 0, 0);
      const u32x4 vt = {cvtpk(d0[0], d0[1]), cvtpk(d0[2], d0[3]), cvtpk(d1[0], d1[1]), cvtpk(d1[2], d1[3])};
      acc[db] = __builtin_amdgcn_mfma_f32_16x16x32_bf16(__builtin_bit_cast(bf16x8, vt), __builtin_bit_cast(bf16x8, pb), acc[db], 0, 0, 0);
    }
  }
#undef LOADSTEP
  l = xrow16_sum(l);
  if (r < 8) {
    float* rec = mrg + (wave * 8 + r) * PREC;
    if (g == 0) { rec[0] = m; rec[1] = l; }
#pragma unroll
    for (int db = 0; db < 8; ++db) *reinterpret_cast<f32x4*>(rec + 4 + db * 16 + g * 4) = acc[db];
  }
  __syncthreads();
  {
    const int h = wave;
    float M = -INFINITY;
#pragma unroll
    for (int w = 0; w < AW; ++w) M = fmaxf(M, mrg[(w * 8 + h) * PREC]);
    float L = 0.f, o0 = 0.f, o1 = 0.f;
#pragma unroll
    for (int w = 0; w < AW; ++w) {
      const float* rec = mrg + (w * 8 + h) * PREC;
      const float f = __builtin_amdgcn_exp2f((rec[0] - M) * LOG2E);
      L = fmaf(f, rec[1], L); o0 = fmaf(f, rec[4 + 2 * lane], o0); o1 = fmaf(f, rec[5 + 2 * lane], o1);
    }
    float* out = reinterpret_cast<float*>(ws + WS_PART) + ((size_t)(kvh * NSPLIT + sp) * 8 + h) * PREC;
    if (lane == 0) *reinterpret_cast<float2*>(out) = make_float2(M, L);
    *reinterpret_cast<float2*>(out + 4 + 2 * lane) = make_float2(o0, o1);
  }
}

__global__ void __launch_bounds__(256) k_wo(const u16* __restrict__ x, const u16* __restrict__ wo, unsigned char* ws) {
  __shared__ float wsc[64], os[2][128], red[4][128];
  __shared__ unsigned lastf;
  const int b = blockIdx.x, h = b >> 3, cg = b & 7;
  const int lane = threadIdx.x & 63, wave = threadIdx.x >> 6;
  const int rq = lane >> 4, cl = lane & 15;
  u32x4 w[8];
#pragma unroll
  for (int i = 0; i < 8; ++i) w[i] = ldnt(wo + (size_t)(h * 128 + wave * 32 + i * 4 + rq) * 1024 + cg * 128 + cl * 8);
  PIN();
  constexpr float LOG2E = 1.4426950408889634f;
  const float* part = reinterpret_cast<const float*>(ws + WS_PART) + ((size_t)(h >> 3) * NSPLIT * 8 + (h & 7)) * PREC;
  if (wave == 0) {
    const float2 ml = *reinterpret_cast<const float2*>(part + (size_t)lane * 8 * PREC);
    float M = ml.x;
#pragma unroll
    for (int o = 32; o >= 1; o >>= 1) M = fmaxf(M, __shfl_xor(M, o));
    const float f = __builtin_amdgcn_exp2f((ml.x - M) * LOG2E);
    float L = f * ml.y;
#pragma unroll
    for (int o = 32; o >= 1; o >>= 1) L += __shfl_xor(L, o);
    wsc[lane] = f / L;
  }
  __syncthreads();
  {
    const int d = threadIdx.x & 127, hf = threadIdx.x >> 7;
    float o = 0.f;
#pragma unroll 8
    for (int s = hf * 32; s < hf * 32 + 32; ++s) o = fmaf(wsc[s], part[(size_t)s * 8 * PREC + 4 + d], o);
    os[hf][d] = o;
  }
  __syncthreads();
  float acc[8] = {0, 0, 0, 0, 0, 0, 0, 0};
#pragma unroll
  for (int i = 0; i < 8; ++i) { const int d = wave * 32 + i * 4 + rq; fma8(acc, bfr(os[0][d] + os[1][d]), w[i]); }
#pragma unroll
  for (int j = 0; j < 8; ++j) acc[j] = xrow16_sum(acc[j]);
  if (rq == 0) {
#pragma unroll
    for (int j = 0; j < 8; ++j) red[wave][cl * 8 + j] = acc[j];
  }
  __syncthreads();
  float* wop = reinterpret_cast<float*>(ws + WS_WOP);
  if (threadIdx.x < 128) {
    const int cidx = threadIdx.x;
    st_ag(wop + h * 1024 + cg * 128 + cidx, (red[0][cidx] + red[1][cidx]) + (red[2][cidx] + red[3][cidx]));
  }
  asm volatile("s_waitcnt vmcnt(0)" ::: "memory");
  __syncthreads();
  if (threadIdx.x == 0) {
    const unsigned old = __hip_atomic_fetch_add(reinterpret_cast<unsigned*>(ws + WS_CNT) + cg, 1u, __ATOMIC_RELAXED, __HIP_MEMORY_SCOPE_AGENT);
    lastf = (old == 31u);
  }
  __syncthreads();
  if (!lastf) return;
  if (threadIdx.x < 128) {
    const int col = cg * 128 + threadIdx.x;
    float v[32];
#pragma unroll
    for (int i = 0; i < 32; ++i) v[i] = ld_ag(wop + i * 1024 + col);
    float s = 0.f;
#pragma unroll
    for (int i = 0; i < 32; ++i) s += v[i];
    const float a = bfr(s);
    const float ga = reinterpret_cast<const float*>(ws + WS_MOD)[2048 + col];
    reinterpret_cast<float*>(ws + WS_X1)[col] = bfr(bf2f(x[col]) + bfr(ga * a));
  }
}

__global__ void __launch_bounds__(256) k_ffn1(const u16* __restrict__ wg, const u16* __restrict__ wu, unsigned char* ws) {
  __shared__ float xs[1024], hs[1024], red[64];
  const int b = blockIdx.x, cg = (b & 7) * 28 + (b >> 3);
  const int lane = threadIdx.x & 63, wave = threadIdx.x >> 6;
  const int rin = lane >> 1, half = lane & 1;
  u32x4 w1[8], w2[8];
#pragma unroll
  for (int i = 0; i < 8; ++i) {
    const size_t off = (size_t)(wave * 256 + i * 32 + rin) * FF + cg * 16 + half * 8;
    w1[i] = ldnt(wg + off); w2[i] = ldnt(wu + off);
  }
  PIN();
  const float* x1 = reinterpret_cast<const float*>(ws + WS_X1);
#pragma unroll
  for (int i = 0; i < 4; ++i) xs[threadIdx.x + 256 * i] = x1[threadIdx.x + 256 * i];
  __syncthreads();
  const float* mod = reinterpret_cast<const float*>(ws + WS_MOD);
  ln_mod(xs, mod + 3072, mod + 4096, hs, red);
  float a1[8] = {0, 0, 0, 0, 0, 0, 0, 0}, a2[8] = {0, 0, 0, 0, 0, 0, 0, 0};
#pragma unroll
  for (int i = 0; i < 8; ++i) { const float hv = hs[wave * 256 + i * 32 + rin]; fma8(a1, hv, w1[i]); fma8(a2, hv, w2[i]); }
  const float gt = reduce16(a1, red);
  const float up = reduce16(a2, red);
  if (threadIdx.x < 16) {
    const float gb = bfr(gt), ub = bfr(up);
    const float inner = 0.7978845608028654f * (gb + 0.044715f * gb * gb * gb);
    const float ge = bfr(0.5f * gb * (1.f + tanhf(inner)));
    reinterpret_cast<float*>(ws + WS_ACT)[cg * 16 + threadIdx.x] = bfr(ge * ub);
  }
}

__global__ void __launch_bounds__(256) k_ffn2(const u16* __restrict__ wd, unsigned char* ws, u16* __restrict__ out) {
  __shared__ float red[64];
  __shared__ unsigned lastf;
  const int b = blockIdx.x, i8 = b >> 3, cg = (b & 7) * 8 + (i8 & 7), ks = i8 >> 3;
  const int lane = threadIdx.x & 63, wave = threadIdx.x >> 6;
  const int rin = lane >> 1, half = lane & 1;
  const int r0 = ks * 896 + wave * 224 + rin;
  u32x4 w[7];
#pragma unroll
  for (int i = 0; i < 7; ++i) w[i] = ldnt(wd + (size_t)(r0 + i * 32) * 1024 + cg * 16 + half * 8);
  PIN();
  const float* act = reinterpret_cast<const float*>(ws + WS_ACT);
  float acc[8] = {0, 0, 0, 0, 0, 0, 0, 0};
#pragma unroll
  for (int i = 0; i < 7; ++i) fma8(acc, act[r0 + i * 32], w[i]);
  const float r = reduce16(acc, red);
  float* dnp = reinterpret_cast<float*>(ws + WS_DNP);
  if (threadIdx.x < 16) st_ag(dnp + ks * 1024 + cg * 16 + threadIdx.x, r);
  asm volatile("s_waitcnt vmcnt(0)" ::: "memory");
  __syncthreads();
  if (threadIdx.x == 0) {
    const unsigned old = __hip_atomic_fetch_add(reinterpret_cast<unsigned*>(ws + WS_CNT) + 8 + cg, 1u, __ATOMIC_RELAXED, __HIP_MEMORY_SCOPE_AGENT);
    lastf = (old == 3u);
  }
  __syncthreads();
  if (!lastf) return;
  if (threadIdx.x < 16) {
    const int col = cg * 16 + threadIdx.x;
    const float v0 = ld_ag(dnp + col), v1 = ld_ag(dnp + 1024 + col), v2 = ld_ag(dnp + 2048 + col), v3 = ld_ag(dnp + 3072 + col);
    const float dn = bfr((v0 + v1) + (v2 + v3));
    const float gb = reinterpret_cast<const float*>(ws + WS_MOD)[5120 + col];
    const float x1 = reinterpret_cast<const float*>(ws + WS_X1)[col];
    const float o = x1 + bfr(gb * dn);
    const __bf16 ob = (__bf16)o;
    out[col] = __builtin_bit_cast(u16, ob);
  }
}

extern "C" void kernel_launch(void* const* d_in, const int* in_sizes, int n_in, void* d_out, int out_size, void* d_ws, size_t ws_size,
                              hipStream_t stream) {
  const u16* x = (const u16*)d_in[0];
  const u16* kc = (const u16*)d_in[1];
  const u16* vc = (const u16*)d_in[2];
  const u16* wq = (const u16*)d_in[3];
  const u16* wo = (const u16*)d_in[4];
  const u16* wg = (const u16*)d_in[6];
  const u16* wu = (const u16*)d_in[7];
  const u16* wd = (const u16*)d_in[8];
  const u16* c = (const u16*)d_in[9];
  const u16* wmod = (const u16*)d_in[10];
  unsigned char* ws = (unsigned char*)d_ws;
  k_mod<<<384, 256, 0, stream>>>(c, wmod, ws);
  k_q<<<256, 256, 0, stream>>>(x, wq, ws);
  k_attn<<<256, 512, 0, stream>>>(kc, vc, ws);
  k_wo<<<256, 256, 0, stream>>>(x, wo, ws);
  k_ffn1<<<224, 256, 0, stream>>>(wg, wu, ws);
  k_ffn2<<<256, 256, 0, stream>>>(wd, ws, (u16*)d_out);
}
```

```cpp
#include <hip/hip_runtime.h>
#include <hip/hip_bf16.h>
#include <stdint.h>

#define GAS __attribute__((address_space(1)))
#define LAS __attribute__((address_space(3)))
typedef unsigned short u16;
typedef unsigned long long u64;
typedef GAS u64 gu64;
using u32x4 = __attribute__((ext_vector_type(4))) unsigned;
using f32x4 = __attribute__((ext_vector_type(4))) float;
using bf16x8 = __attribute__((ext_vector_type(8))) __bf16;
#define RLX_AGENT __ATOMIC_RELAXED, __HIP_MEMORY_SCOPE_AGENT

#define STAMPS 0

constexpr int SK = 65536, DH = 128, FF = 3584;
constexpr int NWG = 256, NTHR = 320;
constexpr int PREC = 132;

constexpr size_t WS_EPOCH = 0;
constexpr size_t WS_FLAG0 = 4096;
constexpr size_t WS_FLAGC = 8192;
constexpr size_t WS_GA = 16384;
constexpr size_t WS_GB = WS_GA + 4 * 2048 * 8;
constexpr size_t WS_GQ = WS_GB + 4 * 4096 * 8;
constexpr size_t WS_GW = WS_GQ + 4 * 4096 * 8;
constexpr size_t WS_GX = WS_GW + 32 * 1024 * 8;
constexpr size_t WS_GG = WS_GX + 1024 * 8;
constexpr size_t WS_GU = WS_GG + 8 * FF * 8;
constexpr size_t WS_GD = WS_GU + 8 * FF * 8;
constexpr size_t WS_PART = WS_GD + 32 * 1024 * 8;
constexpr size_t WS_STAMP = WS_PART + (size_t)256 * 8 * PREC * 4;

constexpr int RING = 0, SLOT = 16384, NSLOT = 8;
constexpr int L_FULL = NSLOT * SLOT, L_FREE = L_FULL + 64, L_CBAR = L_FULL + 128, L_DEAD = L_FULL + 132;
constexpr int L_RED = L_FULL + 256;
constexpr int L_V1 = L_RED + 2048;
constexpr int L_V2 = L_V1 + 4096;
constexpr int L_MRG = L_V2 + 4096;
constexpr int LDS_BYTES = L_MRG + 4 * 8 * PREC * 4;

constexpr long long TCAP = 2000000;

__device__ __forceinline__ float bf2f(u16 v) { return __uint_as_float(((unsigned)v) << 16); }
__device__ __forceinline__ float bfr(float x) { unsigned u = __float_as_uint(x); u = (u + 0x7fffu + ((u >> 16) & 1u)) & 0xffff0000u; return __uint_as_float(u); }
__device__ __forceinline__ float lo16(unsigned w) { return __uint_as_float(w << 16); }
__device__ __forceinline__ float hi16(unsigned w) { return __uint_as_float(w & 0xffff0000u); }
__device__ __forceinline__ unsigned cvtpk(float lo, float hi) {
  typedef __attribute__((ext_vector_type(2))) __bf16 bf16x2_t;
  typedef __attribute__((ext_vector_type(2))) float f32x2_t;
  const f32x2_t v = {lo, hi};
  return __builtin_bit_cast(unsigned, __builtin_convertvector(v, bf16x2_t));
}
__device__ __forceinline__ u64 rt() { return __builtin_amdgcn_s_memrealtime(); }
__device__ __forceinline__ float xrow16_max(float x) {
  auto s = __builtin_amdgcn_permlane16_swap(__float_as_uint(x), __float_as_uint(x), false, false);
  x = fmaxf(__uint_as_float(s[0]), __uint_as_float(s[1]));
  auto t = __builtin_amdgcn_permlane32_swap(__float_as_uint(x), __float_as_uint(x), false, false);
  return fmaxf(__uint_as_float(t[0]), __uint_as_float(t[1]));
}
__device__ __forceinline__ float xrow16_sum(float x) {
  auto s = __builtin_amdgcn_permlane16_swap(__float_as_uint(x), __float_as_uint(x), false, false);
  x = __uint_as_float(s[0]) + __uint_as_float(s[1]);
  auto t = __builtin_amdgcn_permlane32_swap(__float_as_uint(x), __float_as_uint(x), false, false);
  return __uint_as_float(t[0]) + __uint_as_float(t[1]);
}
__device__ __forceinline__ void fma8(float (&acc)[8], float xv, u32x4 w) {
#pragma unroll
  for (int k = 0; k < 4; ++k) { acc[2 * k] = fmaf(xv, lo16(w[k]), acc[2 * k]); acc[2 * k + 1] = fmaf(xv, hi16(w[k]), acc[2 * k + 1]); }
}
__device__ __forceinline__ float wave_sum(float v) {
#pragma unroll
  for (int o = 32; o >= 1; o >>= 1) v += __shfl_xor(v, o);
  return v;
}
__device__ __forceinline__ void st_gran(gu64* g, float v) { __hip_atomic_store(g, (1ull << 32) | (u64)__float_as_uint(v), RLX_AGENT); }
__device__ __forceinline__ void st_u64(gu64* g, u64 v) { __hip_atomic_store(g, v, RLX_AGENT); }
__device__ __forceinline__ u64 ld_u64(const gu64* g) { return __hip_atomic_load(g, RLX_AGENT); }
#define VM_DRAIN() asm volatile("s_waitcnt vmcnt(0)" ::: "memory")
#define LDS_WAIT() asm volatile("s_waitcnt lgkmcnt(0)" ::: "memory")
#define GLDS(src, dst) __builtin_amdgcn_global_load_lds((const unsigned*)(src), (unsigned*)(dst), 16, 0, 2)

struct Args {
  const u16* x; const u16* kc; const u16* vc; const u16* wq; const u16* wo; const u16* wg; const u16* wu; const u16* wd;
  const u16* c; const u16* wmod; unsigned char* ws; u16* out;
};

__global__ void __launch_bounds__(NTHR, 1) fused_kernel(Args a) {
  __shared__ __attribute__((aligned(16))) unsigned char lds[LDS_BYTES];
  LAS unsigned char* L8 = (LAS unsigned char*)lds;
  volatile LAS unsigned* FULL = (volatile LAS unsigned*)(L8 + L_FULL);
  volatile LAS unsigned* FREE = (volatile LAS unsigned*)(L8 + L_FREE);
  volatile LAS unsigned* CBAR = (volatile LAS unsigned*)(L8 + L_CBAR);
  volatile LAS unsigned* DEAD = (volatile LAS unsigned*)(L8 + L_DEAD);
  const int tid = threadIdx.x, lane = tid & 63, wave = __builtin_amdgcn_readfirstlane(tid >> 6), b = blockIdx.x;
  if (tid < 64) ((LAS unsigned*)(L8 + L_FULL))[tid] = 0u;
  __syncthreads();
  const bool ffn = b < 224;
  const int QD = ffn ? 43 : 39, NFILL = QD + 2;
  unsigned char* ws = a.ws;

  if (wave == 0) {
    const int cg = b & 63, ks = b >> 6;
    int koff[4];
#pragma unroll
    for (int j = 0; j < 4; ++j) { const int row = 4 * j + (lane >> 4); koff[j] = row * 256 + (((lane & 15) ^ row) << 4); }
    const unsigned char* kb = (const unsigned char*)a.kc + ((size_t)(b >> 6) * SK + (size_t)(b & 63) * 1024) * 256;
    const unsigned char* vb = (const unsigned char*)a.vc + ((size_t)(b >> 6) * SK + (size_t)(b & 63) * 1024) * 256;
    int pub = 0; bool dead = false;
    for (int q = 0; q < NFILL; ++q) {
      const int s = q & 7;
      if (q >= NSLOT) {
        const unsigned need = (unsigned)(q - NSLOT + 1);
        if (FREE[s] < need && pub < q) { VM_DRAIN(); for (; pub < q; ++pub) FULL[pub & 7] = (unsigned)(pub + 1); }
        const u64 t0 = rt(); int sp = 0;
        while (FREE[s] < need) { __builtin_amdgcn_s_sleep(1); if ((++sp & 255) == 0 && (*DEAD != 0u || (long long)(rt() - t0) > TCAP)) { dead = true; break; } }
        if (dead) { *DEAD = 1u; break; }
        __builtin_amdgcn_fence(__ATOMIC_ACQUIRE, "workgroup");
      }
      unsigned char* dst = lds + s * SLOT;
      if (q >= 5 && q < 37) {
        const size_t o = (size_t)(q - 5) * 8192;
#pragma unroll
        for (int j = 0; j < 8; ++j) GLDS(kb + o + koff[j & 3] + (j >> 2) * 4096, dst + j * 1024);
#pragma unroll
        for (int j = 0; j < 8; ++j) GLDS(vb + o + koff[j & 3] + (j >> 2) * 4096, dst + 8192 + j * 1024);
      } else {
        const unsigned char* src; size_t step; int n = 16;
        if (q == 0) { src = (const unsigned char*)(a.wmod + (size_t)(256 * ks + (lane >> 2)) * 6144 + 32 * cg + (lane & 3) * 8); step = (size_t)16 * 6144 * 2; }
        else if (q < 3) { src = (const unsigned char*)(a.wq + (size_t)(256 * ks + 128 * (q - 1) + (lane >> 3)) * 4096 + 64 * cg + (lane & 7) * 8); step = (size_t)8 * 4096 * 2; }
        else if (q < 5) { src = (const unsigned char*)(a.wmod + (size_t)(256 * ks + 128 * (q - 3) + (lane >> 3)) * 6144 + 2048 + 64 * cg + (lane & 7) * 8); step = (size_t)8 * 6144 * 2; }
        else if (q < 39) { src = (const unsigned char*)(a.wo + (size_t)(128 * (b >> 3) + 64 * (q - 37) + (lane >> 4)) * 1024 + 128 * (b & 7) + (lane & 15) * 8); step = (size_t)4 * 1024 * 2; }
        else if (q < QD) { const u16* w = (q < 41) ? a.wg : a.wu; const int hf = (q - 39) & 1;
          src = (const unsigned char*)(w + (size_t)(128 * (b / 28) + 64 * hf + (lane >> 4)) * FF + 128 * (b % 28) + (lane & 15) * 8); step = (size_t)4 * FF * 2; }
        else { src = (const unsigned char*)(a.wd + (size_t)(112 * (b >> 3) + 64 * (q - QD) + (lane >> 4)) * 1024 + 128 * (b & 7) + (lane & 15) * 8); step = (size_t)4 * 1024 * 2; if (q == QD + 1) n = 12; }
#pragma unroll
        for (int j = 0; j < 12; ++j) GLDS(src + j * step, dst + j * 1024);
        if (n == 16) {
#pragma unroll
          for (int j = 12; j < 16; ++j) GLDS(src + j * step, dst + j * 1024);
        }
      }
      if (q + 1 - pub > 2 && q + 1 < NFILL) {
        asm volatile("s_waitcnt vmcnt(32)" ::: "memory");
        FULL[pub & 7] = (unsigned)(pub + 1); ++pub;
      }
    }
    VM_DRAIN();
    if (!dead) for (; pub < NFILL; ++pub) FULL[pub & 7] = (unsigned)(pub + 1);
    return;
  }

  const int cw = wave - 1, ct = tid - 64;
  LAS float* RED = (LAS float*)(L8 + L_RED);
  LAS float* V1 = (LAS float*)(L8 + L_V1);
  LAS float* V2 = (LAS float*)(L8 + L_V2);
  LAS float* MRG = (LAS float*)(L8 + L_MRG);
  unsigned cepoch = 0;
#define STAMP() do {} while (0)
  auto spin = [&](u64 t0, int& sp) -> bool {
    __builtin_amdgcn_s_sleep(1);
    if ((++sp & 127) != 0) return false;
    if (*DEAD != 0u) return true;
    if ((long long)(rt() - t0) > TCAP) { *DEAD = 1u; return true; }
    return false;
  };
  auto cbar = [&]() {
    LDS_WAIT();
    cepoch += 4;
    if (lane == 0) __hip_atomic_fetch_add((LAS unsigned*)CBAR, 1u, __ATOMIC_RELAXED, __HIP_MEMORY_SCOPE_WORKGROUP);
    const u64 t0 = rt(); int sp = 0;
    while (*CBAR < cepoch) { if (spin(t0, sp)) break; }
    asm volatile("" ::: "memory");
  };
  auto wait_full = [&](int q) {
    const u64 t0 = rt(); int sp = 0;
    while (FULL[q & 7] < (unsigned)(q + 1)) { if (spin(t0, sp)) break; }
    asm volatile("" ::: "memory");
  };
#define GATHER(N, v, ADDR)                                                                      \
  do { const u64 t0_ = rt(); int sp_ = 0;                                                        \
    for (;;) { bool ok_ = true; u64 x_[N];                                                       \
      _Pragma("unroll") for (int k = 0; k < N; ++k) x_[k] = ld_u64(ADDR);                        \
      _Pragma("unroll") for (int k = 0; k < N; ++k) { v[k] = __uint_as_float((unsigned)x_[k]); ok_ &= (unsigned)(x_[k] >> 32) == 1u; } \
      if (__all(ok_)) break;                                                                     \
      if (spin(t0_, sp_)) break; }                                                               \
  } while (0)

  STAMP();
  const u64 T = ld_u64((gu64*)(ws + WS_EPOCH)) + 1ull;
  {
    const int cg = b & 63, ks = b >> 6;
    if (ct < 32) st_u64((gu64*)(ws + WS_GA) + ks * 2048 + cg * 32 + ct, 0);
    if (ct < 64) { st_u64((gu64*)(ws + WS_GB) + ks * 4096 + cg * 64 + ct, 0); st_u64((gu64*)(ws + WS_GQ) + ks * 4096 + cg * 64 + ct, 0); }
    if (ct == 64) st_u64((gu64*)(ws + WS_FLAGC) + b, 0);
    if (ct < 128) {
      st_u64((gu64*)(ws + WS_GW) + (b >> 3) * 1024 + (b & 7) * 128 + ct, 0);
      st_u64((gu64*)(ws + WS_GD) + (b >> 3) * 1024 + (b & 7) * 128 + ct, 0);
      if (ffn) { st_u64((gu64*)(ws + WS_GG) + (b / 28) * FF + (b % 28) * 128 + ct, 0); st_u64((gu64*)(ws + WS_GU) + (b / 28) * FF + (b % 28) * 128 + ct, 0); }
    }
    if (b < 64 && ct >= 128 && ct < 144) st_u64((gu64*)(ws + WS_GX) + b * 16 + (ct - 128), 0);
    VM_DRAIN();
    cbar();
    if (ct == 0) st_u64((gu64*)(ws + WS_FLAG0) + b, T);
  }
  float mean1, sd1;
  {
    float xv[16]; float s = 0.f;
#pragma unroll
    for (int i = 0; i < 16; ++i) { xv[i] = bf2f(a.x[lane + 64 * i]); s += xv[i]; }
    const float mean = wave_sum(s) * (1.f / 1024.f);
    float qv = 0.f;
#pragma unroll
    for (int i = 0; i < 16; ++i) { const float d = xv[i] - mean; qv = fmaf(d, d, qv); }
    const float var = wave_sum(qv) * (1.f / 1024.f);
    mean1 = bfr(mean); sd1 = bfr(sqrtf(bfr(bfr(var) + 1e-5f)));
  }
  {
    const int ks = b >> 6, cg = b & 63;
    float sv[4];
#pragma unroll
    for (int i = 0; i < 4; ++i) { const float cv = bf2f(a.c[256 * ks + 64 * cw + 16 * i + (lane >> 2)]); sv[i] = bfr(cv / (1.f + __expf(-cv))); }
    wait_full(0);
    float acc[8] = {0, 0, 0, 0, 0, 0, 0, 0};
    const LAS u32x4* t = (const LAS u32x4*)(L8 + RING + 0 * SLOT + cw * 4096 + lane * 16);
#pragma unroll
    for (int i = 0; i < 4; ++i) fma8(acc, sv[i], t[i * 64]);
#pragma unroll
    for (int j = 0; j < 8; ++j) { acc[j] += __shfl_xor(acc[j], 4); acc[j] += __shfl_xor(acc[j], 8); acc[j] = xrow16_sum(acc[j]); }
    if (lane < 4) {
#pragma unroll
      for (int j = 0; j < 8; ++j) RED[cw * 128 + lane * 8 + j] = acc[j];
    }
    cbar();
    if (ct == 0) FREE[0] = 1u;
    if (ct < 32) st_gran((gu64*)(ws + WS_GA) + ks * 2048 + cg * 32 + ct, (RED[ct] + RED[128 + ct]) + (RED[256 + ct] + RED[384 + ct]));
  }
  STAMP();
  {
    const gu64* f = (const gu64*)(ws + WS_FLAG0);
    const u64 t0 = rt(); int sp = 0;
    for (;;) {
      const u64 v0 = ld_u64(f + lane), v1 = ld_u64(f + 64 + lane), v2 = ld_u64(f + 128 + lane), v3 = ld_u64(f + 192 + lane);
      if (__all(v0 == T && v1 == T && v2 == T && v3 == T)) break;
      if (spin(t0, sp)) break;
    }
    if (b == 0 && ct == 0) st_u64((gu64*)(ws + WS_EPOCH), T);
  }
  STAMP();
  {
    const int ks = b >> 6;
    const gu64* g = (const gu64*)(ws + WS_GA) + 256 * ks + ct;
    float v[8];
    GATHER(8, v, g + (k & 3) * 2048 + (k >> 2) * 1024);
    const float sa = bfr((v[0] + v[1]) + (v[2] + v[3])), ca = bfr((v[4] + v[5]) + (v[6] + v[7]));
    const float xv = bf2f(a.x[256 * ks + ct]);
    const float ln = bfr(bfr(xv - mean1) / sd1);
    V2[ct] = bfr(bfr(ln * bfr(1.f + ca)) + sa);
    cbar();
  }
  STAMP();
#pragma unroll
  for (int ph = 0; ph < 2; ++ph) {
    const int ks = b >> 6, cg = b & 63;
    float acc[8] = {0, 0, 0, 0, 0, 0, 0, 0};
#pragma unroll
    for (int hf = 0; hf < 2; ++hf) {
      const int q = 1 + 2 * ph + hf;
      float xv[4];
#pragma unroll
      for (int i = 0; i < 4; ++i) {
        const int row = 128 * hf + 32 * cw + 8 * i + (lane >> 3);
        if (ph == 0) xv[i] = V2[row];
        else { const float cv = bf2f(a.c[256 * ks + row]); xv[i] = bfr(cv / (1.f + __expf(-cv))); }
      }
      wait_full(q);
      const LAS u32x4* t = (const LAS u32x4*)(L8 + RING + q * SLOT + cw * 4096 + lane * 16);
#pragma unroll
      for (int i = 0; i < 4; ++i) fma8(acc, xv[i], t[i * 64]);
    }
#pragma unroll
    for (int j = 0; j < 8; ++j) { acc[j] += __shfl_xor(acc[j], 8); acc[j] = xrow16_sum(acc[j]); }
    if (lane < 8) {
#pragma unroll
      for (int j = 0; j < 8; ++j) RED[cw * 128 + lane * 8 + j] = acc[j];
    }
    cbar();
    if (ct == 0) { FREE[1 + 2 * ph] = 2 + 2 * ph; FREE[2 + 2 * ph] = 3 + 2 * ph; }
    if (ct < 64) st_gran((gu64*)(ws + (ph == 0 ? WS_GQ : WS_GB)) + ks * 4096 + cg * 64 + ct, (RED[ct] + RED[128 + ct]) + (RED[256 + ct] + RED[384 + ct]));
    cbar();
    if (ph == 0) STAMP();
  }
  STAMP();
  const int kvh = b >> 6;
  {
    const gu64* g = (const gu64*)(ws + WS_GQ) + 1024 * kvh + ct;
    float v[16];
    GATHER(16, v, g + (k & 3) * 4096 + (k >> 2) * 256);
#pragma unroll
    for (int i = 0; i < 4; ++i) V1[ct + 256 * i] = bfr((v[4 * i] + v[4 * i + 1]) + (v[4 * i + 2] + v[4 * i + 3]));
    cbar();
  }
  STAMP();
  constexpr float SCALE = 0.08838834764831845f, LOG2E = 1.4426950408889634f;
  {
    const int r = lane & 15, g = lane >> 4;
    u32x4 qf[4];
#pragma unroll
    for (int s = 0; s < 4; ++s) {
      const LAS f32x4* qp = (const LAS f32x4*)(V1 + (r & 7) * DH + s * 32 + g * 8);
      const f32x4 p0 = qp[0], p1 = qp[1];
      u32x4 v = {cvtpk(p0[0], p0[1]), cvtpk(p0[2], p0[3]), cvtpk(p1[0], p1[1]), cvtpk(p1[2], p1[3])};
      if (r >= 8) v = u32x4{0u, 0u, 0u, 0u};
      qf[s] = v;
    }
    u32x4 sel[2];
#pragma unroll
    for (int h = 0; h < 2; ++h) {
      const int kk = r + 16 * h - 8 * g;
#pragma unroll
      for (int w = 0; w < 4; ++w) sel[h][w] = (kk == 2 * w ? 0x3f80u : 0u) | (kk == 2 * w + 1 ? 0x3f800000u : 0u);
    }
    int loff[4];
#pragma unroll
    for (int s = 0; s < 4; ++s) loff[s] = r * 256 + (((4 * s + g) ^ r) << 4);
    f32x4 acc[8];
#pragma unroll
    for (int i = 0; i < 8; ++i) acc[i] = f32x4{0.f, 0.f, 0.f, 0.f};
    float m = -INFINITY, l = 0.f;
    for (int st = cw; st < 32; st += 4) {
      const int q = 5 + st;
      wait_full(q);
      const LAS unsigned char* sb = L8 + RING + (q & 7) * SLOT;
      u32x4 kf[8], vf[8];
#pragma unroll
      for (int t = 0; t < 2; ++t)
#pragma unroll
        for (int s = 0; s < 4; ++s) {
          kf[t * 4 + s] = *(const LAS u32x4*)(sb + loff[s] + t * 4096);
          vf[t * 4 + s] = *(const LAS u32x4*)(sb + loff[s] + t * 4096 + 8192);
        }
      LDS_WAIT();
      if (lane == 0) FREE[q & 7] = (unsigned)(q + 1);
      asm volatile("" : "+v"(kf[0]), "+v"(kf[1]), "+v"(kf[2]), "+v"(kf[3]), "+v"(kf[4]), "+v"(kf[5]), "+v"(kf[6]), "+v"(kf[7]),
                        "+v"(vf[0]), "+v"(vf[1]), "+v"(vf[2]), "+v"(vf[3]), "+v"(vf[4]), "+v"(vf[5]), "+v"(vf[6]), "+v"(vf[7]) : : "memory");
      f32x4 s0 = {0.f, 0.f, 0.f, 0.f}, s1 = {0.f, 0.f, 0.f, 0.f};
#pragma unroll
      for (int s = 0; s < 4; ++s) {
        s0 = __builtin_amdgcn_mfma_f32_16x16x32_bf16(__builtin_bit_cast(bf16x8, kf[s]), __builtin_bit_cast(bf16x8, qf[s]), s0, 0, 0, 0);
        s1 = __builtin_amdgcn_mfma_f32_16x16x32_bf16(__builtin_bit_cast(bf16x8, kf[4 + s]), __builtin_bit_cast(bf16x8, qf[s]), s1, 0, 0, 0);
      }
      float t[8];
#pragma unroll
      for (int j = 0; j < 4; ++j) { t[j] = bfr(bfr(s0[j]) * SCALE); t[4 + j] = bfr(bfr(s1[j]) * SCALE); }
      float mx = t[0];
#pragma unroll
      for (int j = 1; j < 8; ++j) mx = fmaxf(mx, t[j]);
      mx = xrow16_max(mx);
      const float mn = fmaxf(m, mx);
      const float alpha = __builtin_amdgcn_exp2f((m - mn) * LOG2E);
      m = mn;
      float p[8], ps = 0.f;
#pragma unroll
      for (int j = 0; j < 8; ++j) { p[j] = __builtin_amdgcn_exp2f((t[j] - mn) * LOG2E); ps += p[j]; }
      l = fmaf(l, alpha, ps);
      const u32x4 pb = {cvtpk(p[0], p[1]), cvtpk(p[2], p[3]), cvtpk(p[4], p[5]), cvtpk(p[6], p[7])};
#pragma unroll
      for (int i = 0; i < 8; ++i) acc[i] *= alpha;
#pragma unroll
      for (int db = 0; db < 8; ++db) {
        const f32x4 z = {0.f, 0.f, 0.f, 0.f};
        const f32x4 d0 = __builtin_amdgcn_mfma_f32_16x16x32_bf16(__builtin_bit_cast(bf16x8, vf[db >> 1]), __builtin_bit_cast(bf16x8, sel[db & 1]), z, 0, 0, 0);
        const f32x4 d1 = __builtin_amdgcn_mfma_f32_16x16x32_bf16(__builtin_bit_cast(bf16x8, vf[4 + (db >> 1)]), __builtin_bit_cast(bf16x8, sel[db & 1]), z, 0, 0, 0);
        const u32x4 vt = {cvtpk(d0[0], d0[1]), cvtpk(d0[2], d0[3]), cvtpk(d1[0], d1[1]), cvtpk(d1[2], d1[3])};
        acc[db] = __builtin_amdgcn_mfma_f32_16x16x32_bf16(__builtin_bit_cast(bf16x8, vt), __builtin_bit_cast(bf16x8, pb), acc[db], 0, 0, 0);
      }
    }
    l = xrow16_sum(l);
    if (r < 8) {
      LAS float* rec = MRG + (cw * 8 + r) * PREC;
      if (g == 0) { rec[0] = m; rec[1] = l; }
#pragma unroll
      for (int db = 0; db < 8; ++db) *(LAS f32x4*)(rec + 4 + db * 16 + g * 4) = acc[db];
    }
    cbar();
    STAMP();
#pragma unroll
    for (int hh = 0; hh < 2; ++hh) {
      const int h = 2 * cw + hh;
      float M = -INFINITY;
#pragma unroll
      for (int w = 0; w < 4; ++w) M = fmaxf(M, MRG[(w * 8 + h) * PREC]);
      float Ls = 0.f, o0 = 0.f, o1 = 0.f;
#pragma unroll
      for (int w = 0; w < 4; ++w) {
        const LAS float* rec = MRG + (w * 8 + h) * PREC;
        const float f = __builtin_amdgcn_exp2f((rec[0] - M) * LOG2E);
        Ls = fmaf(f, rec[1], Ls); o0 = fmaf(f, rec[4 + 2 * lane], o0); o1 = fmaf(f, rec[5 + 2 * lane], o1);
      }
      gu64* out = (gu64*)(ws + WS_PART + ((size_t)b * 8 + h) * PREC * 4);
      if (lane == 0) st_u64(out, ((u64)__float_as_uint(Ls) << 32) | __float_as_uint(M));
      st_u64(out + 2 + lane, ((u64)__float_as_uint(o1) << 32) | __float_as_uint(o0));
    }
    VM_DRAIN();
    cbar();
    if (ct == 0) st_u64((gu64*)(ws + WS_FLAGC) + b, 1ull);
  }
  STAMP();
  float sbv = 0.f, cbv = 0.f, gav = 0.f, gbv = 0.f;
  if (ffn && ct < 128) {
    const gu64* g = (const gu64*)(ws + WS_GB) + 1024 + 128 * (b / 28) + ct;
    float v[8];
    GATHER(8, v, g + (k & 3) * 4096 + (k >> 2) * 1024);
    sbv = bfr((v[0] + v[1]) + (v[2] + v[3])); cbv = bfr((v[4] + v[5]) + (v[6] + v[7]));
  }
  if (b < 64 && cw == 3) {
    const gu64* g = (const gu64*)(ws + WS_GB) + 16 * b + (lane & 15);
    float v[8];
    GATHER(8, v, g + (k & 3) * 4096 + (k >> 2) * 3072);
    gav = bfr((v[0] + v[1]) + (v[2] + v[3])); gbv = bfr((v[4] + v[5]) + (v[6] + v[7]));
  }
  {
    const int h = b >> 3, kv = h >> 3;
    const gu64* f = (const gu64*)(ws + WS_FLAGC) + 64 * kv;
    { const u64 t0 = rt(); int sp = 0;
      for (;;) { const u64 v = ld_u64(f + lane); if (__all(v == 1ull)) break; if (spin(t0, sp)) break; } }
    asm volatile("" ::: "memory");
    const gu64* pbase = (const gu64*)(ws + WS_PART + ((size_t)kv * 64 * 8 + (h & 7)) * PREC * 4);
    const u64 ml = ld_u64(pbase + (size_t)lane * 4 * PREC);
    u64 ov[16];
#pragma unroll
    for (int i = 0; i < 16; ++i) ov[i] = ld_u64(pbase + (size_t)(16 * cw + i) * 4 * PREC + 2 + lane);
    const float Ml = __uint_as_float((unsigned)ml), Ll = __uint_as_float((unsigned)(ml >> 32));
    float M = Ml;
#pragma unroll
    for (int o = 32; o >= 1; o >>= 1) M = fmaxf(M, __shfl_xor(M, o));
    const float f1 = __builtin_amdgcn_exp2f((Ml - M) * LOG2E);
    const float Lt = wave_sum(f1 * Ll);
    const float wgt = f1 / Lt;
    float o0 = 0.f, o1 = 0.f;
#pragma unroll
    for (int i = 0; i < 16; ++i) {
      const float wv = __shfl(wgt, 16 * cw + i);
      o0 = fmaf(wv, __uint_as_float((unsigned)ov[i]), o0); o1 = fmaf(wv, __uint_as_float((unsigned)(ov[i] >> 32)), o1);
    }
    RED[cw * 128 + 2 * lane] = o0; RED[cw * 128 + 2 * lane + 1] = o1;
    cbar();
    if (ct < 128) V2[ct] = bfr((RED[ct] + RED[128 + ct]) + (RED[256 + ct] + RED[384 + ct]));
    cbar();
  }
  STAMP();
#define TILE256(ACC, q0, XS, n2)                                                                 \
  _Pragma("unroll") for (int hf = 0; hf < 2; ++hf) {                                              \
    wait_full((q0) + hf);                                                                         \
    const LAS u32x4* t_ = (const LAS u32x4*)(L8 + RING + (((q0) + hf) & 7) * SLOT + cw * 4096 + lane * 16); \
    _Pragma("unroll") for (int i = 0; i < 4; ++i) {                                               \
      const int row_ = 64 * hf + 16 * cw + 4 * i + (lane >> 4);                                   \
      if (hf == 0 || 16 * cw + 4 * i < (n2)) fma8(ACC, XS[row_], t_[i * 64]); } }
  {
    float acc[8] = {0, 0, 0, 0, 0, 0, 0, 0};
    TILE256(acc, 37, V2, 64)
#pragma unroll
    for (int j = 0; j < 8; ++j) acc[j] = xrow16_sum(acc[j]);
    if (lane < 16) {
#pragma unroll
      for (int j = 0; j < 8; ++j) RED[cw * 128 + lane * 8 + j] = acc[j];
    }
    cbar();
    if (ct == 0) { FREE[37 & 7] = 38u; FREE[38 & 7] = 39u; }
    if (ct < 128) st_gran((gu64*)(ws + WS_GW) + (b >> 3) * 1024 + (b & 7) * 128 + ct, (RED[ct] + RED[128 + ct]) + (RED[256 + ct] + RED[384 + ct]));
  }
  STAMP();
  float x1r = 0.f;
  if (b < 64 && cw == 3) {
    const gu64* g = (const gu64*)(ws + WS_GW) + (size_t)(lane >> 4) * 1024 + 16 * b + (lane & 15);
    float v[8];
    GATHER(8, v, g + k * 4096);
    float s = ((v[0] + v[1]) + (v[2] + v[3])) + ((v[4] + v[5]) + (v[6] + v[7]));
    s = xrow16_sum(s);
    x1r = bfr(bf2f(a.x[16 * b + (lane & 15)]) + bfr(gav * bfr(s)));
    if (lane < 16) st_gran((gu64*)(ws + WS_GX) + 16 * b + lane, x1r);
  }
  if (ffn) {
    {
      const gu64* g = (const gu64*)(ws + WS_GX) + lane;
      float v[16];
      GATHER(16, v, g + k * 64);
      float s = 0.f;
#pragma unroll
      for (int i = 0; i < 16; ++i) s += v[i];
      const float mean = wave_sum(s) * (1.f / 1024.f);
      float qv = 0.f;
#pragma unroll
      for (int i = 0; i < 16; ++i) { const float d = v[i] - mean; qv = fmaf(d, d, qv); }
      const float var = wave_sum(qv) * (1.f / 1024.f);
      const float mb = bfr(mean), sd = bfr(sqrtf(bfr(bfr(var) + 1e-5f)));
      if (cw == 0) {
#pragma unroll
        for (int i = 0; i < 16; ++i) V1[lane + 64 * i] = v[i];
      }
      cbar();
      if (ct < 128) {
        const float xv = V1[128 * (b / 28) + ct];
        const float ln = bfr(bfr(xv - mb) / sd);
        V2[ct] = bfr(bfr(ln * bfr(1.f + cbv)) + sbv);
      }
      cbar();
    }
    STAMP();
    float ag[8] = {0, 0, 0, 0, 0, 0, 0, 0}, au[8] = {0, 0, 0, 0, 0, 0, 0, 0};
    TILE256(ag, 39, V2, 64)
    TILE256(au, 41, V2, 64)
#pragma unroll
    for (int j = 0; j < 8; ++j) { ag[j] = xrow16_sum(ag[j]); au[j] = xrow16_sum(au[j]); }
    if (lane < 16) {
#pragma unroll
      for (int j = 0; j < 8; ++j) { RED[cw * 128 + lane * 8 + j] = ag[j]; MRG[cw * 128 + lane * 8 + j] = au[j]; }
    }
    cbar();
    if (ct == 0) { FREE[39 & 7] = 40u; FREE[40 & 7] = 41u; FREE[41 & 7] = 42u; FREE[42 & 7] = 43u; }
    if (ct < 128) st_gran((gu64*)(ws + WS_GG) + (b / 28) * FF + (b % 28) * 128 + ct, (RED[ct] + RED[128 + ct]) + (RED[256 + ct] + RED[384 + ct]));
    else { const int c2 = ct - 128; st_gran((gu64*)(ws + WS_GU) + (b / 28) * FF + (b % 28) * 128 + c2, (MRG[c2] + MRG[128 + c2]) + (MRG[256 + c2] + MRG[384 + c2])); }
    cbar();
  }
  STAMP();
  {
    const int col = 112 * (b >> 3) + (ct < 112 ? ct : (ct < 224 ? ct - 112 : 0));
    const gu64* g = (const gu64*)(ws + ((ct >= 112 && ct < 224) ? WS_GU : WS_GG)) + col;
    float v[8];
    GATHER(8, v, g + k * FF);
    const float s = bfr(((v[0] + v[1]) + (v[2] + v[3])) + ((v[4] + v[5]) + (v[6] + v[7])));
    if (ct < 224) V1[ct] = s;
    cbar();
    if (ct < 112) {
      const float gb_ = V1[ct], ub = V1[112 + ct];
      const float inner = 0.7978845608028654f * (gb_ + 0.044715f * gb_ * gb_ * gb_);
      const float ge = bfr(0.5f * gb_ * (1.f + tanhf(inner)));
      V2[ct] = bfr(ge * ub);
    }
    cbar();
  }
  STAMP();
  {
    float acc[8] = {0, 0, 0, 0, 0, 0, 0, 0};
    TILE256(acc, QD, V2, 48)
#pragma unroll
    for (int j = 0; j < 8; ++j) acc[j] = xrow16_sum(acc[j]);
    if (lane < 16) {
#pragma unroll
      for (int j = 0; j < 8; ++j) RED[cw * 128 + lane * 8 + j] = acc[j];
    }
    cbar();
    if (ct < 128) st_gran((gu64*)(ws + WS_GD) + (b >> 3) * 1024 + (b & 7) * 128 + ct, (RED[ct] + RED[128 + ct]) + (RED[256 + ct] + RED[384 + ct]));
  }
  STAMP();
  if (b < 64 && cw == 3) {
    const gu64* g = (const gu64*)(ws + WS_GD) + (size_t)(lane >> 4) * 1024 + 16 * b + (lane & 15);
    float v[8];
    GATHER(8, v, g + k * 4096);
    float s = ((v[0] + v[1]) + (v[2] + v[3])) + ((v[4] + v[5]) + (v[6] + v[7]));
    s = xrow16_sum(s);
    const float o = x1r + bfr(gbv * bfr(s));
    if (lane < 16) a.out[16 * b + lane] = (u16)(__float_as_uint(bfr(o)) >> 16);
  }
  STAMP();
}

extern "C" void kernel_launch(void* const* d_in, const int* in_sizes, int n_in, void* d_out, int out_size, void* d_ws, size_t ws_size,
                              hipStream_t stream) {
  Args a;
  a.x = (const u16*)d_in[0]; a.kc = (const u16*)d_in[1]; a.vc = (const u16*)d_in[2]; a.wq = (const u16*)d_in[3]; a.wo = (const u16*)d_in[4];
  a.wg = (const u16*)d_in[6]; a.wu = (const u16*)d_in[7]; a.wd = (const u16*)d_in[8]; a.c = (const u16*)d_in[9]; a.wmod = (const u16*)d_in[10];
  a.ws = (unsigned char*)d_ws; a.out = (u16*)d_out;
  fused_kernel<<<NWG, NTHR, 0, stream>>>(a);
}
```

```cpp
#include <hip/hip_runtime.h>
#include <hip/hip_bf16.h>
#include <stdint.h>

#define GAS __attribute__((address_space(1)))
#define LAS __attribute__((address_space(3)))
typedef unsigned short u16;
typedef unsigned long long u64;
typedef GAS u64 gu64;
using u32x4 = __attribute__((ext_vector_type(4))) unsigned;
using f32x4 = __attribute__((ext_vector_type(4))) float;
using bf16x8 = __attribute__((ext_vector_type(8))) __bf16;
#define RLX_AGENT __ATOMIC_RELAXED, __HIP_MEMORY_SCOPE_AGENT

#define GOP 2
#define GOSIG(n) do { if (GOP == (n) && lane == 0) __hip_atomic_fetch_add((LAS unsigned*)GO, 1u, __ATOMIC_RELAXED, __HIP_MEMORY_SCOPE_WORKGROUP); } while (0)
#define STAMPS 0

constexpr int SK = 65536, DH = 128, FF = 3584;
constexpr int NWG = 256, NTHR = 320;
constexpr int PREC = 132;

constexpr size_t WS_EPOCH = 0;
constexpr size_t WS_FLAG0 = 4096;
constexpr size_t WS_FLAGC = 8192;
constexpr size_t WS_GA = 16384;
constexpr size_t WS_GB = WS_GA + 4 * 2048 * 8;
constexpr size_t WS_GQ = WS_GB + 4 * 4096 * 8;
constexpr size_t WS_GW = WS_GQ + 4 * 4096 * 8;
constexpr size_t WS_GX = WS_GW + 32 * 1024 * 8;
constexpr size_t WS_GG = WS_GX + 1024 * 8;
constexpr size_t WS_GU = WS_GG + 8 * FF * 8;
constexpr size_t WS_GD = WS_GU + 8 * FF * 8;
constexpr size_t WS_PART = WS_GD + 32 * 1024 * 8;
constexpr size_t WS_STAMP = WS_PART + (size_t)256 * 8 * PREC * 4;

constexpr int RING = 0, SLOT = 16384, NSLOT = 8;
constexpr int L_FULL = NSLOT * SLOT, L_FREE = L_FULL + 64, L_CBAR = L_FULL + 128, L_DEAD = L_FULL + 132, L_GO = L_FULL + 136;
constexpr int L_RED = L_FULL + 256;
constexpr int L_V1 = L_RED + 2048;
constexpr int L_V2 = L_V1 + 4096;
constexpr int L_MRG = L_V2 + 4096;
constexpr int LDS_BYTES = L_MRG + 4 * 8 * PREC * 4;

constexpr long long TCAP = 2000000;

__device__ __forceinline__ float bf2f(u16 v) { return __uint_as_float(((unsigned)v) << 16); }
__device__ __forceinline__ float bfr(float x) { unsigned u = __float_as_uint(x); u = (u + 0x7fffu + ((u >> 16) & 1u)) & 0xffff0000u; return __uint_as_float(u); }
__device__ __forceinline__ float lo16(unsigned w) { return __uint_as_float(w << 16); }
__device__ __forceinline__ float hi16(unsigned w) { return __uint_as_float(w & 0xffff0000u); }
__device__ __forceinline__ unsigned cvtpk(float lo, float hi) {
  typedef __attribute__((ext_vector_type(2))) __bf16 bf16x2_t;
  typedef __attribute__((ext_vector_type(2))) float f32x2_t;
  const f32x2_t v = {lo, hi};
  return __builtin_bit_cast(unsigned, __builtin_convertvector(v, bf16x2_t));
}
__device__ __forceinline__ u64 rt() { return __builtin_amdgcn_s_memrealtime(); }
__device__ __forceinline__ float xrow16_max(float x) {
  auto s = __builtin_amdgcn_permlane16_swap(__float_as_uint(x), __float_as_uint(x), false, false);
  x = fmaxf(__uint_as_float(s[0]), __uint_as_float(s[1]));
  auto t = __builtin_amdgcn_permlane32_swap(__float_as_uint(x), __float_as_uint(x), false, false);
  return fmaxf(__uint_as_float(t[0]), __uint_as_float(t[1]));
}
__device__ __forceinline__ float xrow16_sum(float x) {
  auto s = __builtin_amdgcn_permlane16_swap(__float_as_uint(x), __float_as_uint(x), false, false);
  x = __uint_as_float(s[0]) + __uint_as_float(s[1]);
  auto t = __builtin_amdgcn_permlane32_swap(__float_as_uint(x), __float_as_uint(x), false, false);
  return __uint_as_float(t[0]) + __uint_as_float(t[1]);
}
__device__ __forceinline__ void fma8(float (&acc)[8], float xv, u32x4 w) {
#pragma unroll
  for (int k = 0; k < 4; ++k) { acc[2 * k] = fmaf(xv, lo16(w[k]), acc[2 * k]); acc[2 * k + 1] = fmaf(xv, hi16(w[k]), acc[2 * k + 1]); }
}
__device__ __forceinline__ float wave_sum(float v) {
#pragma unroll
  for (int o = 32; o >= 1; o >>= 1) v += __shfl_xor(v, o);
  return v;
}
__device__ __forceinline__ void st_gran(gu64* g, float v) { __hip_atomic_store(g, (1ull << 32) | (u64)__float_as_uint(v), RLX_AGENT); }
__device__ __forceinline__ void st_u64(gu64* g, u64 v) { __hip_atomic_store(g, v, RLX_AGENT); }
__device__ __forceinline__ u64 ld_u64(const gu64* g) { return __hip_atomic_load(g, RLX_AGENT); }
__device__ __forceinline__ u32x4 ldnt(const void* p) { return __builtin_nontemporal_load(reinterpret_cast<const u32x4*>(p)); }
#define PIN() do { asm volatile("" ::: "memory"); __builtin_amdgcn_sched_barrier(0); } while (0)
#define VM_DRAIN() asm volatile("s_waitcnt vmcnt(0)" ::: "memory")
#define LDS_WAIT() asm volatile("s_waitcnt lgkmcnt(0)" ::: "memory")
#define GLDS(src, dst) __builtin_amdgcn_global_load_lds((const unsigned*)(src), (unsigned*)(dst), 16, 0, 2)

struct Args {
  const u16* x; const u16* kc; const u16* vc; const u16* wq; const u16* wo; const u16* wg; const u16* wu; const u16* wd;
  const u16* c; const u16* wmod; unsigned char* ws; u16* out;
};

__global__ void __launch_bounds__(NTHR, 1) fused_kernel(Args a) {
  __shared__ __attribute__((aligned(16))) unsigned char lds[LDS_BYTES];
  LAS unsigned char* L8 = (LAS unsigned char*)lds;
  volatile LAS unsigned* FULL = (volatile LAS unsigned*)(L8 + L_FULL);
  volatile LAS unsigned* FREE = (volatile LAS unsigned*)(L8 + L_FREE);
  volatile LAS unsigned* CBAR = (volatile LAS unsigned*)(L8 + L_CBAR);
  volatile LAS unsigned* DEAD = (volatile LAS unsigned*)(L8 + L_DEAD);
  volatile LAS unsigned* GO = (volatile LAS unsigned*)(L8 + L_GO);
  const int tid = threadIdx.x, lane = tid & 63, wave = __builtin_amdgcn_readfirstlane(tid >> 6), b = blockIdx.x;
  if (tid < 64) ((LAS unsigned*)(L8 + L_FULL))[tid] = 0u;
  __syncthreads();
  const bool ffn = b < 224;
  const int QD = ffn ? 38 : 34, NFILL = QD + 2;
  unsigned char* ws = a.ws;

  if (wave == 0) {
    int koff[4];
#pragma unroll
    for (int j = 0; j < 4; ++j) { const int row = 4 * j + (lane >> 4); koff[j] = row * 256 + (((lane & 15) ^ row) << 4); }
    const unsigned char* kb = (const unsigned char*)a.kc + ((size_t)(b >> 6) * SK + (size_t)(b & 63) * 1024) * 256;
    const unsigned char* vb = (const unsigned char*)a.vc + ((size_t)(b >> 6) * SK + (size_t)(b & 63) * 1024) * 256;
    int pub = 0; bool dead = false;
    { int sp = 0; while (*GO < 4u && ++sp < 10000000) __builtin_amdgcn_s_sleep(1); }
    for (int q = 0; q < NFILL; ++q) {
      const int s = q & 7;
      if (q >= NSLOT) {
        const unsigned need = (unsigned)(q - NSLOT + 1);
        if (FREE[s] < need && pub < q) { VM_DRAIN(); for (; pub < q; ++pub) FULL[pub & 7] = (unsigned)(pub + 1); }
        const u64 t0 = rt(); int sp = 0;
        while (FREE[s] < need) { __builtin_amdgcn_s_sleep(1); if ((++sp & 255) == 0 && (*DEAD != 0u || (long long)(rt() - t0) > TCAP)) { dead = true; break; } }
        if (dead) { *DEAD = 1u; break; }
        __builtin_amdgcn_fence(__ATOMIC_ACQUIRE, "workgroup");
      }
      unsigned char* dst = lds + s * SLOT;
      if (q < 32) {
        const size_t o = (size_t)q * 8192;
#pragma unroll
        for (int j = 0; j < 8; ++j) GLDS(kb + o + koff[j & 3] + (j >> 2) * 4096, dst + j * 1024);
#pragma unroll
        for (int j = 0; j < 8; ++j) GLDS(vb + o + koff[j & 3] + (j >> 2) * 4096, dst + 8192 + j * 1024);
      } else {
        const unsigned char* src; size_t step; int n = 16;
        if (q < 34) { src = (const unsigned char*)(a.wo + (size_t)(128 * (b >> 3) + 64 * (q - 32) + (lane >> 4)) * 1024 + 128 * (b & 7) + (lane & 15) * 8); step = (size_t)4 * 1024 * 2; }
        else if (q < QD) { const u16* w = (q < 36) ? a.wg : a.wu; const int hf = (q - 34) & 1;
          src = (const unsigned char*)(w + (size_t)(128 * (b / 28) + 64 * hf + (lane >> 4)) * FF + 128 * (b % 28) + (lane & 15) * 8); step = (size_t)4 * FF * 2; }
        else { src = (const unsigned char*)(a.wd + (size_t)(112 * (b >> 3) + 64 * (q - QD) + (lane >> 4)) * 1024 + 128 * (b & 7) + (lane & 15) * 8); step = (size_t)4 * 1024 * 2; if (q == QD + 1) n = 12; }
#pragma unroll
        for (int j = 0; j < 12; ++j) GLDS(src + j * step, dst + j * 1024);
        if (n == 16) {
#pragma unroll
          for (int j = 12; j < 16; ++j) GLDS(src + j * step, dst + j * 1024);
        }
      }
      if (q + 1 - pub > 2 && q + 1 < NFILL) {
        asm volatile("s_waitcnt vmcnt(32)" ::: "memory");
        FULL[pub & 7] = (unsigned)(pub + 1); ++pub;
      }
    }
    VM_DRAIN();
    if (!dead) for (; pub < NFILL; ++pub) FULL[pub & 7] = (unsigned)(pub + 1);
    return;
  }

  const int cw = wave - 1, ct = tid - 64;
  LAS float* RED = (LAS float*)(L8 + L_RED);
  LAS float* V1 = (LAS float*)(L8 + L_V1);
  LAS float* V2 = (LAS float*)(L8 + L_V2);
  LAS float* MRG = (LAS float*)(L8 + L_MRG);
  unsigned cepoch = 0;
#define STAMP() do {} while (0)
  auto spin = [&](u64 t0, int& sp) -> bool {
    if (++sp > 64) __builtin_amdgcn_s_sleep(1);
    if ((sp & 127) != 0) return false;
    if (*DEAD != 0u) return true;
    if ((long long)(rt() - t0) > TCAP) { *DEAD = 1u; return true; }
    return false;
  };
  auto cbar = [&]() {
    LDS_WAIT();
    cepoch += 4;
    if (lane == 0) __hip_atomic_fetch_add((LAS unsigned*)CBAR, 1u, __ATOMIC_RELAXED, __HIP_MEMORY_SCOPE_WORKGROUP);
    const u64 t0 = rt(); int sp = 0;
    while (*CBAR < cepoch) { if (spin(t0, sp)) break; }
    asm volatile("" ::: "memory");
  };
  auto wait_full = [&](int q) {
    const u64 t0 = rt(); int sp = 0;
    while (FULL[q & 7] < (unsigned)(q + 1)) { if (spin(t0, sp)) break; }
    asm volatile("" ::: "memory");
  };
#define GATHER(N, v, ADDR)                                                                      \
  do { const u64 t0_ = rt(); int sp_ = 0;                                                        \
    for (;;) { bool ok_ = true; u64 x_[N];                                                       \
      _Pragma("unroll") for (int k = 0; k < N; ++k) x_[k] = ld_u64(ADDR);                        \
      _Pragma("unroll") for (int k = 0; k < N; ++k) { v[k] = __uint_as_float((unsigned)x_[k]); ok_ &= (unsigned)(x_[k] >> 32) == 1u; } \
      if (__all(ok_)) break;                                                                     \
      if (spin(t0_, sp_)) break; }                                                               \
  } while (0)

  STAMP();
  const int ks = b >> 6, cg = b & 63;
  const u64 Traw = ld_u64((gu64*)(ws + WS_EPOCH));
  const u32x4 xa = *(const u32x4*)(a.x + lane * 8), xb = *(const u32x4*)(a.x + 512 + lane * 8);
  const u16 xs_raw = a.x[256 * ks + ct];
  u16 cA[4], cB[8];
#pragma unroll
  for (int i = 0; i < 4; ++i) cA[i] = a.c[256 * ks + 64 * cw + 16 * i + (lane >> 2)];
#pragma unroll
  for (int i = 0; i < 8; ++i) cB[i] = a.c[256 * ks + 128 * (i >> 2) + 32 * cw + 8 * (i & 3) + (lane >> 3)];
  if (ct < 32) st_u64((gu64*)(ws + WS_GA) + ks * 2048 + cg * 32 + ct, 0);
  if (ct < 64) { st_u64((gu64*)(ws + WS_GB) + ks * 4096 + cg * 64 + ct, 0); st_u64((gu64*)(ws + WS_GQ) + ks * 4096 + cg * 64 + ct, 0); }
  if (ct >= 64 && ct < 68) st_u64((gu64*)(ws + WS_FLAGC) + 4 * b + (ct - 64), 0);
  if (ct < 128) {
    st_u64((gu64*)(ws + WS_GW) + (b >> 3) * 1024 + (b & 7) * 128 + ct, 0);
    st_u64((gu64*)(ws + WS_GD) + (b >> 3) * 1024 + (b & 7) * 128 + ct, 0);
  } else if (ffn) {
    st_u64((gu64*)(ws + WS_GG) + (b / 28) * FF + (b % 28) * 128 + (ct - 128), 0);
    st_u64((gu64*)(ws + WS_GU) + (b / 28) * FF + (b % 28) * 128 + (ct - 128), 0);
  }
  if (b < 64 && ct >= 128 && ct < 144) st_u64((gu64*)(ws + WS_GX) + b * 16 + (ct - 128), 0);
  PIN();
  u32x4 wA[4], wQ[8], wB[8];
#pragma unroll
  for (int i = 0; i < 4; ++i) wA[i] = ldnt(a.wmod + (size_t)(256 * ks + 64 * cw + 16 * i + (lane >> 2)) * 6144 + 32 * cg + (lane & 3) * 8);
  PIN();
  GOSIG(0);
#define LOAD_QB()                                                                                   \
  do { _Pragma("unroll") for (int i = 0; i < 8; ++i) wQ[i] = ldnt(a.wq + (size_t)(256 * ks + 128 * (i >> 2) + 32 * cw + 8 * (i & 3) + (lane >> 3)) * 4096 + 64 * cg + (lane & 7) * 8); \
    _Pragma("unroll") for (int i = 0; i < 8; ++i) wB[i] = ldnt(a.wmod + (size_t)(256 * ks + 128 * (i >> 2) + 32 * cw + 8 * (i & 3) + (lane >> 3)) * 6144 + 2048 + 64 * cg + (lane & 7) * 8); \
    PIN(); } while (0)
  const u64 T = Traw + 1ull;
  float mean1, sd1;
  {
    float xv[16];
#pragma unroll
    for (int k = 0; k < 4; ++k) { xv[2 * k] = lo16(xa[k]); xv[2 * k + 1] = hi16(xa[k]); xv[8 + 2 * k] = lo16(xb[k]); xv[9 + 2 * k] = hi16(xb[k]); }
    float s = 0.f;
#pragma unroll
    for (int i = 0; i < 16; ++i) s += xv[i];
    const float mean = wave_sum(s) * (1.f / 1024.f);
    float qv = 0.f;
#pragma unroll
    for (int i = 0; i < 16; ++i) { const float d = xv[i] - mean; qv = fmaf(d, d, qv); }
    const float var = wave_sum(qv) * (1.f / 1024.f);
    mean1 = bfr(mean); sd1 = bfr(sqrtf(bfr(bfr(var) + 1e-5f)));
  }
  const float ln_s = bfr(bfr(bf2f(xs_raw) - mean1) / sd1);
  float sA[4], sB[8];
#pragma unroll
  for (int i = 0; i < 4; ++i) { const float cv = bf2f(cA[i]); sA[i] = bfr(cv / (1.f + __expf(-cv))); }
#pragma unroll
  for (int i = 0; i < 8; ++i) { const float cv = bf2f(cB[i]); sB[i] = bfr(cv / (1.f + __expf(-cv))); }
  {
    float acc[8] = {0, 0, 0, 0, 0, 0, 0, 0};
#pragma unroll
    for (int i = 0; i < 4; ++i) fma8(acc, sA[i], wA[i]);
#pragma unroll
    for (int j = 0; j < 8; ++j) { acc[j] += __shfl_xor(acc[j], 4); acc[j] += __shfl_xor(acc[j], 8); acc[j] = xrow16_sum(acc[j]); }
    if (lane < 4) {
#pragma unroll
      for (int j = 0; j < 8; ++j) RED[cw * 128 + lane * 8 + j] = acc[j];
    }
    VM_DRAIN();
    cbar();
    if (ct < 32) st_gran((gu64*)(ws + WS_GA) + ks * 2048 + cg * 32 + ct, (RED[ct] + RED[128 + ct]) + (RED[256 + ct] + RED[384 + ct]));
    if (ct == 64) st_u64((gu64*)(ws + WS_FLAG0) + b, T);
    LOAD_QB();
  }
  GOSIG(1);
  STAMP();
  {
    const gu64* f = (const gu64*)(ws + WS_FLAG0);
    const u64 t0 = rt(); int sp = 0;
    for (;;) {
      const u64 v0 = ld_u64(f + lane), v1 = ld_u64(f + 64 + lane), v2 = ld_u64(f + 128 + lane), v3 = ld_u64(f + 192 + lane);
      if (__all(v0 == T && v1 == T && v2 == T && v3 == T)) break;
      if (spin(t0, sp)) break;
    }
    asm volatile("" ::: "memory");
    if (b == 0 && ct == 0) st_u64((gu64*)(ws + WS_EPOCH), T);
  }
  STAMP();
  {
    const gu64* g = (const gu64*)(ws + WS_GA) + 256 * ks + ct;
    float v[8];
    GATHER(8, v, g + (k & 3) * 2048 + (k >> 2) * 1024);
    const float sa = bfr((v[0] + v[1]) + (v[2] + v[3])), ca = bfr((v[4] + v[5]) + (v[6] + v[7]));
    V2[ct] = bfr(bfr(ln_s * bfr(1.f + ca)) + sa);
    cbar();
  }
  GOSIG(2);
  STAMP();
  {
    float aq[8] = {0, 0, 0, 0, 0, 0, 0, 0}, ab[8] = {0, 0, 0, 0, 0, 0, 0, 0};
#pragma unroll
    for (int i = 0; i < 8; ++i) { fma8(aq, V2[128 * (i >> 2) + 32 * cw + 8 * (i & 3) + (lane >> 3)], wQ[i]); fma8(ab, sB[i], wB[i]); }
#pragma unroll
    for (int j = 0; j < 8; ++j) { aq[j] += __shfl_xor(aq[j], 8); aq[j] = xrow16_sum(aq[j]); ab[j] += __shfl_xor(ab[j], 8); ab[j] = xrow16_sum(ab[j]); }
    if (lane < 8) {
#pragma unroll
      for (int j = 0; j < 8; ++j) { RED[cw * 128 + lane * 8 + j] = aq[j]; RED[cw * 128 + 64 + lane * 8 + j] = ab[j]; }
    }
    cbar();
    if (ct < 128) st_gran((gu64*)(ws + (ct < 64 ? WS_GQ : WS_GB)) + ks * 4096 + cg * 64 + (ct & 63), (RED[ct] + RED[128 + ct]) + (RED[256 + ct] + RED[384 + ct]));
  }
  GOSIG(3);
  STAMP();
  const int lr = lane & 15, lg = lane >> 4;
  int loff[4];
#pragma unroll
  for (int s2 = 0; s2 < 4; ++s2) loff[s2] = lr * 256 + (((4 * s2 + lg) ^ lr) << 4);
  u32x4 kf[8], vf[8];
#define LOADKV(q_)                                                                                 \
  do { wait_full(q_);                                                                              \
    const LAS unsigned char* sb_ = L8 + RING + ((q_) & 7) * SLOT;                                  \
    _Pragma("unroll") for (int t_ = 0; t_ < 2; ++t_) _Pragma("unroll") for (int s_ = 0; s_ < 4; ++s_) { \
      kf[t_ * 4 + s_] = *(const LAS u32x4*)(sb_ + loff[s_] + t_ * 4096);                          \
      vf[t_ * 4 + s_] = *(const LAS u32x4*)(sb_ + loff[s_] + t_ * 4096 + 8192); }                 \
    LDS_WAIT();                                                                                    \
    if (lane == 0) FREE[(q_) & 7] = (unsigned)((q_) + 1);                                          \
    asm volatile("" : "+v"(kf[0]), "+v"(kf[1]), "+v"(kf[2]), "+v"(kf[3]), "+v"(kf[4]), "+v"(kf[5]), "+v"(kf[6]), "+v"(kf[7]), \
                      "+v"(vf[0]), "+v"(vf[1]), "+v"(vf[2]), "+v"(vf[3]), "+v"(vf[4]), "+v"(vf[5]), "+v"(vf[6]), "+v"(vf[7]) : : "memory"); \
  } while (0)
  if (GOP < 4) LOADKV(cw);
  STAMP();
  const int kvh = ks;
  {
    const gu64* g = (const gu64*)(ws + WS_GQ) + 1024 * kvh + ct;
    float v[16];
    GATHER(16, v, g + (k & 3) * 4096 + (k >> 2) * 256);
#pragma unroll
    for (int i = 0; i < 4; ++i) V1[ct + 256 * i] = bfr((v[4 * i] + v[4 * i + 1]) + (v[4 * i + 2] + v[4 * i + 3]));
    cbar();
  }
  GOSIG(4);
  if (GOP == 4) LOADKV(cw);
  STAMP();
  constexpr float SCALE = 0.08838834764831845f, LOG2E = 1.4426950408889634f;
  {
    const int r = lane & 15, g = lane >> 4;
    u32x4 qf[4];
#pragma unroll
    for (int s = 0; s < 4; ++s) {
      const LAS f32x4* qp = (const LAS f32x4*)(V1 + (r & 7) * DH + s * 32 + g * 8);
      const f32x4 p0 = qp[0], p1 = qp[1];
      u32x4 v = {cvtpk(p0[0], p0[1]), cvtpk(p0[2], p0[3]), cvtpk(p1[0], p1[1]), cvtpk(p1[2], p1[3])};
      if (r >= 8) v = u32x4{0u, 0u, 0u, 0u};
      qf[s] = v;
    }
    u32x4 sel[2];
#pragma unroll
    for (int h = 0; h < 2; ++h) {
      const int kk = r + 16 * h - 8 * g;
#pragma unroll
      for (int w = 0; w < 4; ++w) sel[h][w] = (kk == 2 * w ? 0x3f80u : 0u) | (kk == 2 * w + 1 ? 0x3f800000u : 0u);
    }
    f32x4 acc[8];
#pragma unroll
    for (int i = 0; i < 8; ++i) acc[i] = f32x4{0.f, 0.f, 0.f, 0.f};
    float m = -INFINITY, l = 0.f;
    for (int st = cw; st < 32; st += 4) {
      if (st != cw) LOADKV(st);
      f32x4 s0 = {0.f, 0.f, 0.f, 0.f}, s1 = {0.f, 0.f, 0.f, 0.f};
#pragma unroll
      for (int s = 0; s < 4; ++s) {
        s0 = __builtin_amdgcn_mfma_f32_16x16x32_bf16(__builtin_bit_cast(bf16x8, kf[s]), __builtin_bit_cast(bf16x8, qf[s]), s0, 0, 0, 0);
        s1 = __builtin_amdgcn_mfma_f32_16x16x32_bf16(__builtin_bit_cast(bf16x8, kf[4 + s]), __builtin_bit_cast(bf16x8, qf[s]), s1, 0, 0, 0);
      }
      float t[8];
#pragma unroll
      for (int j = 0; j < 4; ++j) { t[j] = bfr(bfr(s0[j]) * SCALE); t[4 + j] = bfr(bfr(s1[j]) * SCALE); }
      float mx = t[0];
#pragma unroll
      for (int j = 1; j < 8; ++j) mx = fmaxf(mx, t[j]);
      mx = xrow16_max(mx);
      const float mn = fmaxf(m, mx);
      const float alpha = __builtin_amdgcn_exp2f((m - mn) * LOG2E);
      m = mn;
      float p[8], ps = 0.f;
#pragma unroll
      for (int j = 0; j < 8; ++j) { p[j] = __builtin_amdgcn_exp2f((t[j] - mn) * LOG2E); ps += p[j]; }
      l = fmaf(l, alpha, ps);
      const u32x4 pb = {cvtpk(p[0], p[1]), cvtpk(p[2], p[3]), cvtpk(p[4], p[5]), cvtpk(p[6], p[7])};
#pragma unroll
      for (int i = 0; i < 8; ++i) acc[i] *= alpha;
#pragma unroll
      for (int db = 0; db < 8; ++db) {
        const f32x4 z = {0.f, 0.f, 0.f, 0.f};
        const f32x4 d0 = __builtin_amdgcn_mfma_f32_16x16x32_bf16(__builtin_bit_cast(bf16x8, vf[db >> 1]), __builtin_bit_cast(bf16x8, sel[db & 1]), z, 0, 0, 0);
        const f32x4 d1 = __builtin_amdgcn_mfma_f32_16x16x32_bf16(__builtin_bit_cast(bf16x8, vf[4 + (db >> 1)]), __builtin_bit_cast(bf16x8, sel[db & 1]), z, 0, 0, 0);
        const u32x4 vt = {cvtpk(d0[0], d0[1]), cvtpk(d0[2], d0[3]), cvtpk(d1[0], d1[1]), cvtpk(d1[2], d1[3])};
        acc[db] = __builtin_amdgcn_mfma_f32_16x16x32_bf16(__builtin_bit_cast(bf16x8, vt), __builtin_bit_cast(bf16x8, pb), acc[db], 0, 0, 0);
      }
    }
    l = xrow16_sum(l);
    if (r < 8) {
      LAS float* rec = MRG + (cw * 8 + r) * PREC;
      if (g == 0) { rec[0] = m; rec[1] = l; }
#pragma unroll
      for (int db = 0; db < 8; ++db) *(LAS f32x4*)(rec + 4 + db * 16 + g * 4) = acc[db];
    }
    cbar();
    STAMP();
#pragma unroll
    for (int hh = 0; hh < 2; ++hh) {
      const int h = 2 * cw + hh;
      float M = -INFINITY;
#pragma unroll
      for (int w = 0; w < 4; ++w) M = fmaxf(M, MRG[(w * 8 + h) * PREC]);
      float Ls = 0.f, o0 = 0.f, o1 = 0.f;
#pragma unroll
      for (int w = 0; w < 4; ++w) {
        const LAS float* rec = MRG + (w * 8 + h) * PREC;
        const float f = __builtin_amdgcn_exp2f((rec[0] - M) * LOG2E);
        Ls = fmaf(f, rec[1], Ls); o0 = fmaf(f, rec[4 + 2 * lane], o0); o1 = fmaf(f, rec[5 + 2 * lane], o1);
      }
      gu64* out = (gu64*)(ws + WS_PART + ((size_t)b * 8 + h) * PREC * 4);
      if (lane == 0) st_u64(out, ((u64)__float_as_uint(Ls) << 32) | __float_as_uint(M));
      st_u64(out + 2 + lane, ((u64)__float_as_uint(o1) << 32) | __float_as_uint(o0));
    }
    VM_DRAIN();
    if (lane == 0) st_u64((gu64*)(ws + WS_FLAGC) + 4 * b + cw, 1ull);
  }
  STAMP();
  float sbv = 0.f, cbv = 0.f, gav = 0.f, gbv = 0.f;
  if (ffn && ct < 128) {
    const gu64* g = (const gu64*)(ws + WS_GB) + 1024 + 128 * (b / 28) + ct;
    float v[8];
    GATHER(8, v, g + (k & 3) * 4096 + (k >> 2) * 1024);
    sbv = bfr((v[0] + v[1]) + (v[2] + v[3])); cbv = bfr((v[4] + v[5]) + (v[6] + v[7]));
  }
  if (b < 64 && cw == 3) {
    const gu64* g = (const gu64*)(ws + WS_GB) + 16 * b + (lane & 15);
    float v[8];
    GATHER(8, v, g + (k & 3) * 4096 + (k >> 2) * 3072);
    gav = bfr((v[0] + v[1]) + (v[2] + v[3])); gbv = bfr((v[4] + v[5]) + (v[6] + v[7]));
  }
  {
    const int h = b >> 3, kv = h >> 3;
    const gu64* f = (const gu64*)(ws + WS_FLAGC) + 256 * kv + 4 * lane + ((h & 7) >> 1);
    { const u64 t0 = rt(); int sp = 0;
      for (;;) { const u64 v = ld_u64(f); if (__all(v == 1ull)) break; if (spin(t0, sp)) break; } }
    asm volatile("" ::: "memory");
    const gu64* pbase = (const gu64*)(ws + WS_PART + ((size_t)kv * 64 * 8 + (h & 7)) * PREC * 4);
    const u64 ml = ld_u64(pbase + (size_t)lane * 4 * PREC);
    u64 ov[16];
#pragma unroll
    for (int i = 0; i < 16; ++i) ov[i] = ld_u64(pbase + (size_t)(16 * cw + i) * 4 * PREC + 2 + lane);
    const float Ml = __uint_as_float((unsigned)ml), Ll = __uint_as_float((unsigned)(ml >> 32));
    float M = Ml;
#pragma unroll
    for (int o = 32; o >= 1; o >>= 1) M = fmaxf(M, __shfl_xor(M, o));
    const float f1 = __builtin_amdgcn_exp2f((Ml - M) * LOG2E);
    const float Lt = wave_sum(f1 * Ll);
    const float wgt = f1 / Lt;
    float o0 = 0.f, o1 = 0.f;
#pragma unroll
    for (int i = 0; i < 16; ++i) {
      const float wv = __shfl(wgt, 16 * cw + i);
      o0 = fmaf(wv, __uint_as_float((unsigned)ov[i]), o0); o1 = fmaf(wv, __uint_as_float((unsigned)(ov[i] >> 32)), o1);
    }
    RED[cw * 128 + 2 * lane] = o0; RED[cw * 128 + 2 * lane + 1] = o1;
    cbar();
    if (ct < 128) V2[ct] = bfr((RED[ct] + RED[128 + ct]) + (RED[256 + ct] + RED[384 + ct]));
    cbar();
  }
  STAMP();
#define TILE256(ACC, q0, XS, n2)                                                                 \
  _Pragma("unroll") for (int hf = 0; hf < 2; ++hf) {                                              \
    wait_full((q0) + hf);                                                                         \
    const LAS u32x4* t_ = (const LAS u32x4*)(L8 + RING + (((q0) + hf) & 7) * SLOT + cw * 4096 + lane * 16); \
    _Pragma("unroll") for (int i = 0; i < 4; ++i) {                                               \
      const int row_ = 64 * hf + 16 * cw + 4 * i + (lane >> 4);                                   \
      if (hf == 0 || 16 * cw + 4 * i < (n2)) fma8(ACC, XS[row_], t_[i * 64]); } }
  {
    float acc[8] = {0, 0, 0, 0, 0, 0, 0, 0};
    TILE256(acc, 32, V2, 64)
#pragma unroll
    for (int j = 0; j < 8; ++j) acc[j] = xrow16_sum(acc[j]);
    if (lane < 16) {
#pragma unroll
      for (int j = 0; j < 8; ++j) RED[cw * 128 + lane * 8 + j] = acc[j];
    }
    cbar();
    if (ct == 0) { FREE[32 & 7] = 33u; FREE[33 & 7] = 34u; }
    if (ct < 128) st_gran((gu64*)(ws + WS_GW) + (b >> 3) * 1024 + (b & 7) * 128 + ct, (RED[ct] + RED[128 + ct]) + (RED[256 + ct] + RED[384 + ct]));
  }
  STAMP();
  float x1r = 0.f;
  if (b < 64 && cw == 3) {
    const gu64* g = (const gu64*)(ws + WS_GW) + (size_t)(lane >> 4) * 1024 + 16 * b + (lane & 15);
    float v[8];
    GATHER(8, v, g + k * 4096);
    float s = ((v[0] + v[1]) + (v[2] + v[3])) + ((v[4] + v[5]) + (v[6] + v[7]));
    s = xrow16_sum(s);
    x1r = bfr(bf2f(a.x[16 * b + (lane & 15)]) + bfr(gav * bfr(s)));
    if (lane < 16) st_gran((gu64*)(ws + WS_GX) + 16 * b + lane, x1r);
  }
  if (ffn) {
    {
      const gu64* g = (const gu64*)(ws + WS_GX) + lane;
      float v[16];
      GATHER(16, v, g + k * 64);
      float s = 0.f;
#pragma unroll
      for (int i = 0; i < 16; ++i) s += v[i];
      const float mean = wave_sum(s) * (1.f / 1024.f);
      float qv = 0.f;
#pragma unroll
      for (int i = 0; i < 16; ++i) { const float d = v[i] - mean; qv = fmaf(d, d, qv); }
      const float var = wave_sum(qv) * (1.f / 1024.f);
      const float mb = bfr(mean), sd = bfr(sqrtf(bfr(bfr(var) + 1e-5f)));
      if (cw == 0) {
#pragma unroll
        for (int i = 0; i < 16; ++i) V1[lane + 64 * i] = v[i];
      }
      cbar();
      if (ct < 128) {
        const float xv = V1[128 * (b / 28) + ct];
        const float ln = bfr(bfr(xv - mb) / sd);
        V2[ct] = bfr(bfr(ln * bfr(1.f + cbv)) + sbv);
      }
      cbar();
    }
    STAMP();
    float ag[8] = {0, 0, 0, 0, 0, 0, 0, 0}, au[8] = {0, 0, 0, 0, 0, 0, 0, 0};
    TILE256(ag, 34, V2, 64)
    TILE256(au, 36, V2, 64)
#pragma unroll
    for (int j = 0; j < 8; ++j) { ag[j] = xrow16_sum(ag[j]); au[j] = xrow16_sum(au[j]); }
    if (lane < 16) {
#pragma unroll
      for (int j = 0; j < 8; ++j) { RED[cw * 128 + lane * 8 + j] = ag[j]; MRG[cw * 128 + lane * 8 + j] = au[j]; }
    }
    cbar();
    if (ct < 128) st_gran((gu64*)(ws + WS_GG) + (b / 28) * FF + (b % 28) * 128 + ct, (RED[ct] + RED[128 + ct]) + (RED[256 + ct] + RED[384 + ct]));
    else { const int c2 = ct - 128; st_gran((gu64*)(ws + WS_GU) + (b / 28) * FF + (b % 28) * 128 + c2, (MRG[c2] + MRG[128 + c2]) + (MRG[256 + c2] + MRG[384 + c2])); }
    cbar();
  }
  STAMP();
  {
    const int col = 112 * (b >> 3) + (ct < 112 ? ct : (ct < 224 ? ct - 112 : 0));
    const gu64* g = (const gu64*)(ws + ((ct >= 112 && ct < 224) ? WS_GU : WS_GG)) + col;
    float v[8];
    GATHER(8, v, g + k * FF);
    const float s = bfr(((v[0] + v[1]) + (v[2] + v[3])) + ((v[4] + v[5]) + (v[6] + v[7])));
    if (ct < 224) V1[ct] = s;
    cbar();
    if (ct < 112) {
      const float gb_ = V1[ct], ub = V1[112 + ct];
      const float inner = 0.7978845608028654f * (gb_ + 0.044715f * gb_ * gb_ * gb_);
      const float ge = bfr(0.5f * gb_ * (1.f + tanhf(inner)));
      V2[ct] = bfr(ge * ub);
    }
    cbar();
  }
  STAMP();
  {
    float acc[8] = {0, 0, 0, 0, 0, 0, 0, 0};
    TILE256(acc, QD, V2, 48)
#pragma unroll
    for (int j = 0; j < 8; ++j) acc[j] = xrow16_sum(acc[j]);
    if (lane < 16) {
#pragma unroll
      for (int j = 0; j < 8; ++j) RED[cw * 128 + lane * 8 + j] = acc[j];
    }
    cbar();
    if (ct < 128) st_gran((gu64*)(ws + WS_GD) + (b >> 3) * 1024 + (b & 7) * 128 + ct, (RED[ct] + RED[128 + ct]) + (RED[256 + ct] + RED[384 + ct]));
  }
  STAMP();
  if (b < 64 && cw == 3) {
    const gu64* g = (const gu64*)(ws + WS_GD) + (size_t)(lane >> 4) * 1024 + 16 * b + (lane & 15);
    float v[8];
    GATHER(8, v, g + k * 4096);
    float s = ((v[0] + v[1]) + (v[2] + v[3])) + ((v[4] + v[5]) + (v[6] + v[7]));
    s = xrow16_sum(s);
    const float o = x1r + bfr(gbv * bfr(s));
    if (lane < 16) a.out[16 * b + lane] = (u16)(__float_as_uint(bfr(o)) >> 16);
  }
  STAMP();
}

extern "C" void kernel_launch(void* const* d_in, const int* in_sizes, int n_in, void* d_out, int out_size, void* d_ws, size_t ws_size,
                              hipStream_t stream) {
  Args a;
  a.x = (const u16*)d_in[0]; a.kc = (const u16*)d_in[1]; a.vc = (const u16*)d_in[2]; a.wq = (const u16*)d_in[3]; a.wo = (const u16*)d_in[4];
  a.wg = (const u16*)d_in[6]; a.wu = (const u16*)d_in[7]; a.wd = (const u16*)d_in[8]; a.c = (const u16*)d_in[9]; a.wmod = (const u16*)d_in[10];
  a.ws = (unsigned char*)d_ws; a.out = (u16*)d_out;
  fused_kernel<<<NWG, NTHR, 0, stream>>>(a);
}
```

```cpp
#include <hip/hip_runtime.h>
#include <hip/hip_bf16.h>
#include <stdint.h>

#define GAS __attribute__((address_space(1)))
#define LAS __attribute__((address_space(3)))
typedef unsigned short u16;
typedef unsigned long long u64;
typedef GAS u64 gu64;
using u32x4 = __attribute__((ext_vector_type(4))) unsigned;
using f32x4 = __attribute__((ext_vector_type(4))) float;
using bf16x8 = __attribute__((ext_vector_type(8))) __bf16;
#define RLX_AGENT __ATOMIC_RELAXED, __HIP_MEMORY_SCOPE_AGENT

#define GOP 1
#define GOSIG(n) do { if (GOP == (n) && lane == 0) __hip_atomic_fetch_add((LAS unsigned*)GO, 1u, __ATOMIC_RELAXED, __HIP_MEMORY_SCOPE_WORKGROUP); } while (0)
#define THIN 0
#define STAMPS 0

constexpr int SK = 65536, DH = 128, FF = 3584;
constexpr int NWG = 256, NTHR = 320;
constexpr int PREC = 132;

constexpr size_t WS_EPOCH = 0;
constexpr size_t WS_FLAG0 = 4096;
constexpr size_t WS_FLAGC = 8192;
constexpr size_t WS_GA = 16384;
constexpr size_t WS_GB = WS_GA + 4 * 2048 * 8;
constexpr size_t WS_GQ = WS_GB + 4 * 4096 * 8;
constexpr size_t WS_GW = WS_GQ + 4 * 4096 * 8;
constexpr size_t WS_GX = WS_GW + 32 * 1024 * 8;
constexpr size_t WS_GG = WS_GX + 1024 * 8;
constexpr size_t WS_GU = WS_GG + 8 * FF * 8;
constexpr size_t WS_GD = WS_GU + 8 * FF * 8;
constexpr size_t WS_PART = WS_GD + 32 * 1024 * 8;
constexpr size_t WS_STAMP = WS_PART + (size_t)256 * 8 * PREC * 4;

constexpr int RING = 0, SLOT = 16384, NSLOT = 8;
constexpr int L_FULL = NSLOT * SLOT, L_FREE = L_FULL + 64, L_CBAR = L_FULL + 128, L_DEAD = L_FULL + 132, L_GO = L_FULL + 136, L_CLR = L_FULL + 140;
constexpr int L_RED = L_FULL + 256;
constexpr int L_V1 = L_RED + 2048;
constexpr int L_V2 = L_V1 + 4096;
constexpr int L_MRG = L_V2 + 4096;
constexpr int L_XB = L_MRG + 4 * 8 * PREC * 4;
constexpr int LDS_BYTES = L_XB + 1024;

constexpr long long TCAP = 2000000;

__device__ __forceinline__ float bf2f(u16 v) { return __uint_as_float(((unsigned)v) << 16); }
__device__ __forceinline__ float bfr(float x) { unsigned u = __float_as_uint(x); u = (u + 0x7fffu + ((u >> 16) & 1u)) & 0xffff0000u; return __uint_as_float(u); }
__device__ __forceinline__ float lo16(unsigned w) { return __uint_as_float(w << 16); }
__device__ __forceinline__ float hi16(unsigned w) { return __uint_as_float(w & 0xffff0000u); }
__device__ __forceinline__ unsigned cvtpk(float lo, float hi) {
  typedef __attribute__((ext_vector_type(2))) __bf16 bf16x2_t;
  typedef __attribute__((ext_vector_type(2))) float f32x2_t;
  const f32x2_t v = {lo, hi};
  return __builtin_bit_cast(unsigned, __builtin_convertvector(v, bf16x2_t));
}
__device__ __forceinline__ u64 rt() { return __builtin_amdgcn_s_memrealtime(); }
__device__ __forceinline__ float xrow16_max(float x) {
  auto s = __builtin_amdgcn_permlane16_swap(__float_as_uint(x), __float_as_uint(x), false, false);
  x = fmaxf(__uint_as_float(s[0]), __uint_as_float(s[1]));
  auto t = __builtin_amdgcn_permlane32_swap(__float_as_uint(x), __float_as_uint(x), false, false);
  return fmaxf(__uint_as_float(t[0]), __uint_as_float(t[1]));
}
__device__ __forceinline__ float xrow16_sum(float x) {
  auto s = __builtin_amdgcn_permlane16_swap(__float_as_uint(x), __float_as_uint(x), false, false);
  x = __uint_as_float(s[0]) + __uint_as_float(s[1]);
  auto t = __builtin_amdgcn_permlane32_swap(__float_as_uint(x), __float_as_uint(x), false, false);
  return __uint_as_float(t[0]) + __uint_as_float(t[1]);
}
__device__ __forceinline__ void fma8(float (&acc)[8], float xv, u32x4 w) {
#pragma unroll
  for (int k = 0; k < 4; ++k) { acc[2 * k] = fmaf(xv, lo16(w[k]), acc[2 * k]); acc[2 * k + 1] = fmaf(xv, hi16(w[k]), acc[2 * k + 1]); }
}
__device__ __forceinline__ float wave_sum(float v) {
#pragma unroll
  for (int o = 32; o >= 1; o >>= 1) v += __shfl_xor(v, o);
  return v;
}
__device__ __forceinline__ void st_gran(gu64* g, float v) { __hip_atomic_store(g, (1ull << 32) | (u64)__float_as_uint(v), RLX_AGENT); }
__device__ __forceinline__ void st_u64(gu64* g, u64 v) { __hip_atomic_store(g, v, RLX_AGENT); }
__device__ __forceinline__ u64 ld_u64(const gu64* g) { return __hip_atomic_load(g, RLX_AGENT); }
__device__ __forceinline__ u32x4 ldnt(const void* p) { return __builtin_nontemporal_load(reinterpret_cast<const u32x4*>(p)); }
#define PIN() do { asm volatile("" ::: "memory"); __builtin_amdgcn_sched_barrier(0); } while (0)
#define VM_DRAIN() asm volatile("s_waitcnt vmcnt(0)" ::: "memory")
#define LDS_WAIT() asm volatile("s_waitcnt lgkmcnt(0)" ::: "memory")
#define GLDS(src, dst) __builtin_amdgcn_global_load_lds((const unsigned*)(src), (unsigned*)(dst), 16, 0, 2)

struct Args {
  const u16* x; const u16* kc; const u16* vc; const u16* wq; const u16* wo; const u16* wg; const u16* wu; const u16* wd;
  const u16* c; const u16* wmod; unsigned char* ws; u16* out;
};

__global__ void __launch_bounds__(NTHR, 1) fused_kernel(Args a) {
  __shared__ __attribute__((aligned(16))) unsigned char lds[LDS_BYTES];
  LAS unsigned char* L8 = (LAS unsigned char*)lds;
  volatile LAS unsigned* FULL = (volatile LAS unsigned*)(L8 + L_FULL);
  volatile LAS unsigned* FREE = (volatile LAS unsigned*)(L8 + L_FREE);
  volatile LAS unsigned* CBAR = (volatile LAS unsigned*)(L8 + L_CBAR);
  volatile LAS unsigned* DEAD = (volatile LAS unsigned*)(L8 + L_DEAD);
  volatile LAS unsigned* GO = (volatile LAS unsigned*)(L8 + L_GO);
  volatile LAS unsigned* CLR = (volatile LAS unsigned*)(L8 + L_CLR);
  const int tid = threadIdx.x, lane = tid & 63, wave = __builtin_amdgcn_readfirstlane(tid >> 6), b = blockIdx.x;
  if (tid < 64) ((LAS unsigned*)(L8 + L_FULL))[tid] = 0u;
  __syncthreads();
  const bool ffn = b < 224;
  const int QD = ffn ? 38 : 34, NFILL = QD + 2;
  unsigned char* ws = a.ws;

  if (wave == 0) {
    int koff[4];
#pragma unroll
    for (int j = 0; j < 4; ++j) { const int row = 4 * j + (lane >> 4); koff[j] = row * 256 + (((lane & 15) ^ row) << 4); }
    const unsigned char* kb = (const unsigned char*)a.kc + ((size_t)(b >> 6) * SK + (size_t)(b & 63) * 1024) * 256;
    const unsigned char* vb = (const unsigned char*)a.vc + ((size_t)(b >> 6) * SK + (size_t)(b & 63) * 1024) * 256;
    {
      const int ks = b >> 6, cg = b & 63;
      const u64 T = ld_u64((gu64*)(ws + WS_EPOCH)) + 1ull;
      if (lane < 32) st_u64((gu64*)(ws + WS_GA) + ks * 2048 + cg * 32 + lane, 0);
      st_u64((gu64*)(ws + WS_GB) + ks * 4096 + cg * 64 + lane, 0);
      st_u64((gu64*)(ws + WS_GQ) + ks * 4096 + cg * 64 + lane, 0);
      if (lane < 4) st_u64((gu64*)(ws + WS_FLAGC) + 4 * b + lane, 0);
#pragma unroll
      for (int i = 0; i < 2; ++i) {
        st_u64((gu64*)(ws + WS_GW) + (b >> 3) * 1024 + (b & 7) * 128 + 64 * i + lane, 0);
        st_u64((gu64*)(ws + WS_GD) + (b >> 3) * 1024 + (b & 7) * 128 + 64 * i + lane, 0);
        if (ffn) {
          st_u64((gu64*)(ws + WS_GG) + (b / 28) * FF + (b % 28) * 128 + 64 * i + lane, 0);
          st_u64((gu64*)(ws + WS_GU) + (b / 28) * FF + (b % 28) * 128 + 64 * i + lane, 0);
        }
      }
      if (b < 64 && lane < 8) st_u64((gu64*)(ws + WS_GX) + b * 8 + lane, 0);
      VM_DRAIN();
      *CLR = 1u;
      if (lane == 0) st_u64((gu64*)(ws + WS_FLAG0) + b, T);
    }
    int pub = 0; bool dead = false;
    { int sp = 0; while (*GO < 4u && ++sp < 10000000) __builtin_amdgcn_s_sleep(1); }
    for (int q = 0; q < NFILL; ++q) {
      const int s = q & 7;
      if (q >= NSLOT) {
        const unsigned need = (unsigned)(q - NSLOT + 1);
        if (FREE[s] < need && pub < q) { VM_DRAIN(); for (; pub < q; ++pub) FULL[pub & 7] = (unsigned)(pub + 1); }
        u64 t0 = 0; int sp = 0;
        while (FREE[s] < need) { __builtin_amdgcn_s_sleep(1); if ((++sp & 1023) == 0) { if (*DEAD != 0u) { dead = true; break; } if (t0 == 0) t0 = rt(); else if ((long long)(rt() - t0) > TCAP) { dead = true; break; } } }
        if (dead) { *DEAD = 1u; break; }
        __builtin_amdgcn_fence(__ATOMIC_ACQUIRE, "workgroup");
      }
      unsigned char* dst = lds + s * SLOT;
      if (q < 32) {
        const size_t o = (size_t)q * 8192;
#pragma unroll
        for (int j = 0; j < 8; ++j) GLDS(kb + o + koff[j & 3] + (j >> 2) * 4096, dst + j * 1024);
#pragma unroll
        for (int j = 0; j < 8; ++j) GLDS(vb + o + koff[j & 3] + (j >> 2) * 4096, dst + 8192 + j * 1024);
      } else {
        const unsigned char* src; size_t step; int n = 16;
        if (q < 34) { src = (const unsigned char*)(a.wo + (size_t)(128 * (b >> 3) + 64 * (q - 32) + (lane >> 4)) * 1024 + 128 * (b & 7) + (lane & 15) * 8); step = (size_t)4 * 1024 * 2; }
        else if (q < QD) { const u16* w = (q < 36) ? a.wg : a.wu; const int hf = (q - 34) & 1;
          src = (const unsigned char*)(w + (size_t)(128 * (b / 28) + 64 * hf + (lane >> 4)) * FF + 128 * (b % 28) + (lane & 15) * 8); step = (size_t)4 * FF * 2; }
        else { src = (const unsigned char*)(a.wd + (size_t)(112 * (b >> 3) + 64 * (q - QD) + (lane >> 4)) * 1024 + 128 * (b & 7) + (lane & 15) * 8); step = (size_t)4 * 1024 * 2; if (q == QD + 1) n = 12; }
#pragma unroll
        for (int j = 0; j < 12; ++j) GLDS(src + j * step, dst + j * 1024);
        if (n == 16) {
#pragma unroll
          for (int j = 12; j < 16; ++j) GLDS(src + j * step, dst + j * 1024);
        }
      }
      if (THIN && *GO < 8u) {
        VM_DRAIN();
        for (; pub <= q; ++pub) FULL[pub & 7] = (unsigned)(pub + 1);
      } else if (q + 1 - pub > 2 && q + 1 < NFILL) {
        asm volatile("s_waitcnt vmcnt(32)" ::: "memory");
        FULL[pub & 7] = (unsigned)(pub + 1); ++pub;
      }
    }
    VM_DRAIN();
    if (!dead) for (; pub < NFILL; ++pub) FULL[pub & 7] = (unsigned)(pub + 1);
    return;
  }

  const int cw = wave - 1, ct = tid - 64;
  LAS float* RED = (LAS float*)(L8 + L_RED);
  LAS float* V1 = (LAS float*)(L8 + L_V1);
  LAS float* V2 = (LAS float*)(L8 + L_V2);
  LAS float* MRG = (LAS float*)(L8 + L_MRG);
  unsigned cepoch = 0;
#define STAMP() do {} while (0)
  auto spin = [&](u64& t0, int& sp) -> bool {
    if (++sp > 64) __builtin_amdgcn_s_sleep(1);
    if ((sp & 1023) != 0) return false;
    if (*DEAD != 0u) return true;
    if (t0 == 0) { t0 = rt(); return false; }
    if ((long long)(rt() - t0) > TCAP) { *DEAD = 1u; return true; }
    return false;
  };
  auto cbar = [&]() {
    LDS_WAIT();
    cepoch += 4;
    if (lane == 0) __hip_atomic_fetch_add((LAS unsigned*)CBAR, 1u, __ATOMIC_RELAXED, __HIP_MEMORY_SCOPE_WORKGROUP);
    u64 t0 = 0; int sp = 0;
    while (*CBAR < cepoch) { if (spin(t0, sp)) break; }
    asm volatile("" ::: "memory");
  };
  auto wait_full = [&](int q) {
    u64 t0 = 0; int sp = 0;
    while (FULL[q & 7] < (unsigned)(q + 1)) { if (spin(t0, sp)) break; }
    asm volatile("" ::: "memory");
  };
#define GATHER(N, v, ADDR)                                                                      \
  do { u64 t0_ = 0; int sp_ = 0;                                                        \
    for (;;) { bool ok_ = true; u64 x_[N];                                                       \
      _Pragma("unroll") for (int k = 0; k < N; ++k) x_[k] = ld_u64(ADDR);                        \
      _Pragma("unroll") for (int k = 0; k < N; ++k) { v[k] = __uint_as_float((unsigned)x_[k]); ok_ &= (unsigned)(x_[k] >> 32) == 1u; } \
      if (__all(ok_)) break;                                                                     \
      if (spin(t0_, sp_)) break; }                                                               \
  } while (0)

  STAMP();
  const int ks = b >> 6, cg = b & 63;
  const u64 Traw = ld_u64((gu64*)(ws + WS_EPOCH));
  const u32x4 xa = *(const u32x4*)(a.x + lane * 8), xb = *(const u32x4*)(a.x + 512 + lane * 8);
  const u16 xs_raw = a.x[256 * ks + ct];
  u16 cA[4], cB[8];
#pragma unroll
  for (int i = 0; i < 4; ++i) cA[i] = a.c[256 * ks + 64 * cw + 16 * i + (lane >> 2)];
#pragma unroll
  for (int i = 0; i < 8; ++i) cB[i] = a.c[256 * ks + 128 * (i >> 2) + 32 * cw + 8 * (i & 3) + (lane >> 3)];
  PIN();
  u32x4 wA[4], wQ[8], wB[8];
#pragma unroll
  for (int i = 0; i < 4; ++i) wA[i] = ldnt(a.wmod + (size_t)(256 * ks + 64 * cw + 16 * i + (lane >> 2)) * 6144 + 32 * cg + (lane & 3) * 8);
#pragma unroll
  for (int i = 0; i < 8; ++i) wQ[i] = ldnt(a.wq + (size_t)(256 * ks + 128 * (i >> 2) + 32 * cw + 8 * (i & 3) + (lane >> 3)) * 4096 + 64 * cg + (lane & 7) * 8);
  PIN();
  GOSIG(0);
#define LOAD_QB()                                                                                   \
  do { _Pragma("unroll") for (int i = 0; i < 8; ++i) wB[i] = ldnt(a.wmod + (size_t)(256 * ks + 128 * (i >> 2) + 32 * cw + 8 * (i & 3) + (lane >> 3)) * 6144 + 2048 + 64 * cg + (lane & 7) * 8); \
    PIN(); } while (0)
  const u64 T = Traw + 1ull;
  float mean1, sd1;
  {
    float xv[16];
#pragma unroll
    for (int k = 0; k < 4; ++k) { xv[2 * k] = lo16(xa[k]); xv[2 * k + 1] = hi16(xa[k]); xv[8 + 2 * k] = lo16(xb[k]); xv[9 + 2 * k] = hi16(xb[k]); }
    float s = 0.f;
#pragma unroll
    for (int i = 0; i < 16; ++i) s += xv[i];
    const float mean = wave_sum(s) * (1.f / 1024.f);
    float qv = 0.f;
#pragma unroll
    for (int i = 0; i < 16; ++i) { const float d = xv[i] - mean; qv = fmaf(d, d, qv); }
    const float var = wave_sum(qv) * (1.f / 1024.f);
    mean1 = bfr(mean); sd1 = bfr(sqrtf(bfr(bfr(var) + 1e-5f)));
  }
  const float ln_s = bfr(bfr(bf2f(xs_raw) - mean1) / sd1);
  float sA[4], sB[8];
#pragma unroll
  for (int i = 0; i < 4; ++i) { const float cv = bf2f(cA[i]); sA[i] = bfr(cv / (1.f + __expf(-cv))); }
#pragma unroll
  for (int i = 0; i < 8; ++i) { const float cv = bf2f(cB[i]); sB[i] = bfr(cv / (1.f + __expf(-cv))); }
  {
    float acc[8] = {0, 0, 0, 0, 0, 0, 0, 0};
#pragma unroll
    for (int i = 0; i < 4; ++i) fma8(acc, sA[i], wA[i]);
#pragma unroll
    for (int j = 0; j < 8; ++j) { acc[j] += __shfl_xor(acc[j], 4); acc[j] += __shfl_xor(acc[j], 8); acc[j] = xrow16_sum(acc[j]); }
    if (lane < 4) {
#pragma unroll
      for (int j = 0; j < 8; ++j) RED[cw * 128 + lane * 8 + j] = acc[j];
    }
    { u64 t0 = 0; int sp = 0; while (*CLR == 0u) { if (spin(t0, sp)) break; } }
    cbar();
    if (ct < 32) st_gran((gu64*)(ws + WS_GA) + ks * 2048 + cg * 32 + ct, (RED[ct] + RED[128 + ct]) + (RED[256 + ct] + RED[384 + ct]));
    LOAD_QB();
  }
  GOSIG(1);
  STAMP();
  {
    const gu64* f = (const gu64*)(ws + WS_FLAG0);
    u64 t0 = 0; int sp = 0;
    for (;;) {
      const u64 v0 = ld_u64(f + lane), v1 = ld_u64(f + 64 + lane), v2 = ld_u64(f + 128 + lane), v3 = ld_u64(f + 192 + lane);
      if (__all(v0 == T && v1 == T && v2 == T && v3 == T)) break;
      if (spin(t0, sp)) break;
    }
    asm volatile("" ::: "memory");
    if (b == 0 && ct == 0) st_u64((gu64*)(ws + WS_EPOCH), T);
  }
  STAMP();
  {
    const gu64* g = (const gu64*)(ws + WS_GA) + 256 * ks + ct;
    float v[8];
    GATHER(8, v, g + (k & 3) * 2048 + (k >> 2) * 1024);
    const float sa = bfr((v[0] + v[1]) + (v[2] + v[3])), ca = bfr((v[4] + v[5]) + (v[6] + v[7]));
    V2[ct] = bfr(bfr(ln_s * bfr(1.f + ca)) + sa);
    cbar();
  }
  GOSIG(2);
  STAMP();
  {
    float aq[8] = {0, 0, 0, 0, 0, 0, 0, 0};
#pragma unroll
    for (int i = 0; i < 8; ++i) fma8(aq, V2[128 * (i >> 2) + 32 * cw + 8 * (i & 3) + (lane >> 3)], wQ[i]);
#pragma unroll
    for (int j = 0; j < 8; ++j) { aq[j] += __shfl_xor(aq[j], 8); aq[j] = xrow16_sum(aq[j]); }
    if (lane < 8) {
#pragma unroll
      for (int j = 0; j < 8; ++j) RED[cw * 128 + lane * 8 + j] = aq[j];
    }
    cbar();
    if (ct < 64) st_gran((gu64*)(ws + WS_GQ) + ks * 4096 + cg * 64 + ct, (RED[ct] + RED[128 + ct]) + (RED[256 + ct] + RED[384 + ct]));
    float ab[8] = {0, 0, 0, 0, 0, 0, 0, 0};
#pragma unroll
    for (int i = 0; i < 8; ++i) fma8(ab, sB[i], wB[i]);
#pragma unroll
    for (int j = 0; j < 8; ++j) { ab[j] += __shfl_xor(ab[j], 8); ab[j] = xrow16_sum(ab[j]); }
    if (lane < 8) {
#pragma unroll
      for (int j = 0; j < 8; ++j) RED[cw * 128 + 64 + lane * 8 + j] = ab[j];
    }
    cbar();
    if (ct >= 64 && ct < 128) st_gran((gu64*)(ws + WS_GB) + ks * 4096 + cg * 64 + (ct & 63), (RED[ct] + RED[128 + ct]) + (RED[256 + ct] + RED[384 + ct]));
  }
  GOSIG(3);
  STAMP();
  const int lr = lane & 15, lg = lane >> 4;
  int loff[4];
#pragma unroll
  for (int s2 = 0; s2 < 4; ++s2) loff[s2] = lr * 256 + (((4 * s2 + lg) ^ lr) << 4);
  u32x4 kf[8], vf[8], kn[8], vn[8];
#define LOADKV(kf, vf, q_)                                                                                 \
  do { wait_full(q_);                                                                              \
    const LAS unsigned char* sb_ = L8 + RING + ((q_) & 7) * SLOT;                                  \
    _Pragma("unroll") for (int t_ = 0; t_ < 2; ++t_) _Pragma("unroll") for (int s_ = 0; s_ < 4; ++s_) { \
      kf[t_ * 4 + s_] = *(const LAS u32x4*)(sb_ + loff[s_] + t_ * 4096);                          \
      vf[t_ * 4 + s_] = *(const LAS u32x4*)(sb_ + loff[s_] + t_ * 4096 + 8192); }                 \
    LDS_WAIT();                                                                                    \
    if (lane == 0) FREE[(q_) & 7] = (unsigned)((q_) + 1);                                          \
    asm volatile("" : "+v"(kf[0]), "+v"(kf[1]), "+v"(kf[2]), "+v"(kf[3]), "+v"(kf[4]), "+v"(kf[5]), "+v"(kf[6]), "+v"(kf[7]), \
                      "+v"(vf[0]), "+v"(vf[1]), "+v"(vf[2]), "+v"(vf[3]), "+v"(vf[4]), "+v"(vf[5]), "+v"(vf[6]), "+v"(vf[7]) : : "memory"); \
  } while (0)
  LOADKV(kf, vf, cw);
  LOADKV(kn, vn, cw + 4);
  STAMP();
  const int kvh = ks;
  {
    const gu64* g = (const gu64*)(ws + WS_GQ) + 1024 * kvh + ct;
    float v[16];
    GATHER(16, v, g + (k & 3) * 4096 + (k >> 2) * 256);
#pragma unroll
    for (int i = 0; i < 4; ++i) V1[ct + 256 * i] = bfr((v[4 * i] + v[4 * i + 1]) + (v[4 * i + 2] + v[4 * i + 3]));
    cbar();
  }
  if (lane == 0) __hip_atomic_fetch_add((LAS unsigned*)GO, 1u, __ATOMIC_RELAXED, __HIP_MEMORY_SCOPE_WORKGROUP);
  STAMP();
  constexpr float SCALE = 0.08838834764831845f, LOG2E = 1.4426950408889634f;
  {
    const int r = lane & 15, g = lane >> 4;
    u32x4 qf[4];
#pragma unroll
    for (int s = 0; s < 4; ++s) {
      const LAS f32x4* qp = (const LAS f32x4*)(V1 + (r & 7) * DH + s * 32 + g * 8);
      const f32x4 p0 = qp[0], p1 = qp[1];
      u32x4 v = {cvtpk(p0[0], p0[1]), cvtpk(p0[2], p0[3]), cvtpk(p1[0], p1[1]), cvtpk(p1[2], p1[3])};
      if (r >= 8) v = u32x4{0u, 0u, 0u, 0u};
      qf[s] = v;
    }
    u32x4 sel[2];
#pragma unroll
    for (int h = 0; h < 2; ++h) {
      const int kk = r + 16 * h - 8 * g;
#pragma unroll
      for (int w = 0; w < 4; ++w) sel[h][w] = (kk == 2 * w ? 0x3f80u : 0u) | (kk == 2 * w + 1 ? 0x3f800000u : 0u);
    }
    f32x4 acc[8];
#pragma unroll
    for (int i = 0; i < 8; ++i) acc[i] = f32x4{0.f, 0.f, 0.f, 0.f};
    float m = -INFINITY, l = 0.f;
    auto step = [&](const u32x4 (&kf)[8], const u32x4 (&vf)[8]) {
      f32x4 s0 = {0.f, 0.f, 0.f, 0.f}, s1 = {0.f, 0.f, 0.f, 0.f};
#pragma unroll
      for (int s = 0; s < 4; ++s) {
        s0 = __builtin_amdgcn_mfma_f32_16x16x32_bf16(__builtin_bit_cast(bf16x8, kf[s]), __builtin_bit_cast(bf16x8, qf[s]), s0, 0, 0, 0);
        s1 = __builtin_amdgcn_mfma_f32_16x16x32_bf16(__builtin_bit_cast(bf16x8, kf[4 + s]), __builtin_bit_cast(bf16x8, qf[s]), s1, 0, 0, 0);
      }
      float t[8];
#pragma unroll
      for (int j = 0; j < 4; ++j) { t[j] = bfr(bfr(s0[j]) * SCALE); t[4 + j] = bfr(bfr(s1[j]) * SCALE); }
      float mx = t[0];
#pragma unroll
      for (int j = 1; j < 8; ++j) mx = fmaxf(mx, t[j]);
      mx = xrow16_max(mx);
      const float mn = fmaxf(m, mx);
      const float alpha = __builtin_amdgcn_exp2f((m - mn) * LOG2E);
      m = mn;
      float p[8], ps = 0.f;
#pragma unroll
      for (int j = 0; j < 8; ++j) { p[j] = __builtin_amdgcn_exp2f((t[j] - mn) * LOG2E); ps += p[j]; }
      l = fmaf(l, alpha, ps);
      const u32x4 pb = {cvtpk(p[0], p[1]), cvtpk(p[2], p[3]), cvtpk(p[4], p[5]), cvtpk(p[6], p[7])};
#pragma unroll
      for (int i = 0; i < 8; ++i) acc[i] *= alpha;
#pragma unroll
      for (int db = 0; db < 8; ++db) {
        const f32x4 z = {0.f, 0.f, 0.f, 0.f};
        const f32x4 d0 = __builtin_amdgcn_mfma_f32_16x16x32_bf16(__builtin_bit_cast(bf16x8, vf[db >> 1]), __builtin_bit_cast(bf16x8, sel[db & 1]), z, 0, 0, 0);
        const f32x4 d1 = __builtin_amdgcn_mfma_f32_16x16x32_bf16(__builtin_bit_cast(bf16x8, vf[4 + (db >> 1)]), __builtin_bit_cast(bf16x8, sel[db & 1]), z, 0, 0, 0);
        const u32x4 vt = {cvtpk(d0[0], d0[1]), cvtpk(d0[2], d0[3]), cvtpk(d1[0], d1[1]), cvtpk(d1[2], d1[3])};
        acc[db] = __builtin_amdgcn_mfma_f32_16x16x32_bf16(__builtin_bit_cast(bf16x8, vt), __builtin_bit_cast(bf16x8, pb), acc[db], 0, 0, 0);
      }
    };
    step(kf, vf);
    step(kn, vn);
#pragma unroll 1
    for (int st = cw + 8; st < 32; st += 4) { LOADKV(kf, vf, st); step(kf, vf); }
    l = xrow16_sum(l);
    if (r < 8) {
      LAS float* rec = MRG + (cw * 8 + r) * PREC;
      if (g == 0) { rec[0] = m; rec[1] = l; }
#pragma unroll
      for (int db = 0; db < 8; ++db) *(LAS f32x4*)(rec + 4 + db * 16 + g * 4) = acc[db];
    }
    cbar();
    STAMP();
#pragma unroll
    for (int hh = 0; hh < 2; ++hh) {
      const int h = 2 * cw + hh;
      float M = -INFINITY;
#pragma unroll
      for (int w = 0; w < 4; ++w) M = fmaxf(M, MRG[(w * 8 + h) * PREC]);
      float Ls = 0.f, o0 = 0.f, o1 = 0.f;
#pragma unroll
      for (int w = 0; w < 4; ++w) {
        const LAS float* rec = MRG + (w * 8 + h) * PREC;
        const float f = __builtin_amdgcn_exp2f((rec[0] - M) * LOG2E);
        Ls = fmaf(f, rec[1], Ls); o0 = fmaf(f, rec[4 + 2 * lane], o0); o1 = fmaf(f, rec[5 + 2 * lane], o1);
      }
      gu64* out = (gu64*)(ws + WS_PART + ((size_t)b * 8 + h) * PREC * 4);
      if (lane == 0) st_u64(out, ((u64)__float_as_uint(Ls) << 32) | __float_as_uint(M));
      st_u64(out + 2 + lane, ((u64)__float_as_uint(o1) << 32) | __float_as_uint(o0));
    }
    VM_DRAIN();
    if (lane == 0) st_u64((gu64*)(ws + WS_FLAGC) + 4 * b + cw, 1ull);
  }
  STAMP();
  float sbv[2] = {0.f, 0.f}, cbv[2] = {0.f, 0.f}, gav = 0.f, gbv = 0.f;
  if (ffn && cw == 0) {
    const gu64* g = (const gu64*)(ws + WS_GB) + 1024 + 128 * (b / 28) + 2 * lane;
    float v[16];
    GATHER(16, v, g + (k & 3) * 4096 + ((k >> 2) & 1) + (k >> 3) * 1024);
#pragma unroll
    for (int j = 0; j < 2; ++j) { sbv[j] = bfr((v[4 * j] + v[4 * j + 1]) + (v[4 * j + 2] + v[4 * j + 3])); cbv[j] = bfr((v[8 + 4 * j] + v[9 + 4 * j]) + (v[10 + 4 * j] + v[11 + 4 * j])); }
  }
  if (b < 64 && cw == 3) {
    const gu64* g = (const gu64*)(ws + WS_GB) + 16 * b + (lane & 15);
    float v[8];
    GATHER(8, v, g + (k & 3) * 4096 + (k >> 2) * 3072);
    gav = bfr((v[0] + v[1]) + (v[2] + v[3])); gbv = bfr((v[4] + v[5]) + (v[6] + v[7]));
  }
  LAS u16* XB0 = (LAS u16*)(L8 + L_XB);
  LAS u16* XBW = XB0 + 128 * cw;
  auto gemv_tile = [&](int q0, const LAS u16* xb) -> float {
    typedef __attribute__((ext_vector_type(4))) short s16x4;
    wait_full(q0); wait_full(q0 + 1);
    f32x4 d0 = {0.f, 0.f, 0.f, 0.f}, d1 = {0.f, 0.f, 0.f, 0.f};
    const int toff = (8 * lg + (lr >> 2)) * 256 + (32 * cw + 4 * (lr & 3)) * 2;
#pragma unroll
    for (int s2 = 0; s2 < 4; ++s2) {
      const u32x4 av = *(const LAS u32x4*)(xb + 32 * s2 + 8 * lg);
      const LAS unsigned char* tb = L8 + RING + ((q0 + (s2 >> 1)) & 7) * SLOT + (s2 & 1) * 8192 + toff;
      const s16x4 p0 = __builtin_amdgcn_ds_read_tr16_b64_v4i16((LAS s16x4*)(tb));
      const s16x4 p1 = __builtin_amdgcn_ds_read_tr16_b64_v4i16((LAS s16x4*)(tb + 1024));
      const s16x4 p2 = __builtin_amdgcn_ds_read_tr16_b64_v4i16((LAS s16x4*)(tb + 32));
      const s16x4 p3 = __builtin_amdgcn_ds_read_tr16_b64_v4i16((LAS s16x4*)(tb + 1056));
      typedef __attribute__((ext_vector_type(8))) short s16x8;
      const s16x8 b0 = __builtin_shufflevector(p0, p1, 0, 1, 2, 3, 4, 5, 6, 7), b1 = __builtin_shufflevector(p2, p3, 0, 1, 2, 3, 4, 5, 6, 7);
      d0 = __builtin_amdgcn_mfma_f32_16x16x32_bf16(__builtin_bit_cast(bf16x8, av), __builtin_bit_cast(bf16x8, b0), d0, 0, 0, 0);
      d1 = __builtin_amdgcn_mfma_f32_16x16x32_bf16(__builtin_bit_cast(bf16x8, av), __builtin_bit_cast(bf16x8, b1), d1, 0, 0, 0);
    }
    return (lane & 16) ? d1[0] : d0[0];
  };
  {
    const int h = b >> 3, kv = h >> 3;
    const gu64* f = (const gu64*)(ws + WS_FLAGC) + 256 * kv + 4 * lane + ((h & 7) >> 1);
    { u64 t0 = 0; int sp = 0;
      for (;;) { const u64 v = ld_u64(f); if (__all(v == 1ull)) break; if (spin(t0, sp)) break; } }
    asm volatile("" ::: "memory");
    const gu64* pbase = (const gu64*)(ws + WS_PART + ((size_t)kv * 64 * 8 + (h & 7)) * PREC * 4);
    const u64 ml = ld_u64(pbase + (size_t)lane * 4 * PREC);
    u64 ov[16];
#pragma unroll
    for (int i = 0; i < 16; ++i) ov[i] = ld_u64(pbase + (size_t)(16 * lg + i) * 4 * PREC + 2 + 16 * cw + lr);
    const float Ml = __uint_as_float((unsigned)ml), Ll = __uint_as_float((unsigned)(ml >> 32));
    float M = Ml;
#pragma unroll
    for (int o = 32; o >= 1; o >>= 1) M = fmaxf(M, __shfl_xor(M, o));
    const float f1 = __builtin_amdgcn_exp2f((Ml - M) * LOG2E);
    const float Lt = wave_sum(f1 * Ll);
    const float wgt = f1 / Lt;
    float o0 = 0.f, o1 = 0.f;
#pragma unroll
    for (int i = 0; i < 16; ++i) {
      const float wv = __shfl(wgt, 16 * lg + i);
      o0 = fmaf(wv, __uint_as_float((unsigned)ov[i]), o0); o1 = fmaf(wv, __uint_as_float((unsigned)(ov[i] >> 32)), o1);
    }
    o0 = xrow16_sum(o0); o1 = xrow16_sum(o1);
    if (lane < 16) *(LAS unsigned*)(XB0 + 32 * cw + 2 * lane) = (__float_as_uint(bfr(o0)) >> 16) | (__float_as_uint(bfr(o1)) & 0xffff0000u);
    cbar();
  }
  STAMP();
  {
    const float r = gemv_tile(32, XB0);
    if (lane < 32) st_gran((gu64*)(ws + WS_GW) + (b >> 3) * 1024 + (b & 7) * 128 + 32 * cw + lane, r);
  }
  STAMP();
  float x1r = 0.f;
  if (b < 64 && cw == 3) {
    const gu64* g = (const gu64*)(ws + WS_GW) + (size_t)(lane >> 4) * 1024 + 16 * b + (lane & 15);
    float v[8];
    GATHER(8, v, g + k * 4096);
    float s = ((v[0] + v[1]) + (v[2] + v[3])) + ((v[4] + v[5]) + (v[6] + v[7]));
    s = xrow16_sum(s);
    x1r = bfr(bf2f(a.x[16 * b + (lane & 15)]) + bfr(gav * bfr(s)));
    { const float nb = __shfl_down(x1r, 1);
      if (lane < 16 && !(lane & 1)) st_u64((gu64*)(ws + WS_GX) + 8 * b + (lane >> 1), (1ull << 32) | (u64)((__float_as_uint(x1r) >> 16) | (__float_as_uint(nb) & 0xffff0000u))); }
  }
  if (ffn) {
    cbar();
    if (cw == 0) {
      const gu64* g = (const gu64*)(ws + WS_GX) + lane;
      float v[8];
      GATHER(8, v, g + k * 64);
      float xv[16];
#pragma unroll
      for (int i = 0; i < 8; ++i) { const unsigned u = __float_as_uint(v[i]); xv[2 * i] = lo16(u); xv[2 * i + 1] = hi16(u); }
      float s = 0.f;
#pragma unroll
      for (int i = 0; i < 16; ++i) s += xv[i];
      const float mean = wave_sum(s) * (1.f / 1024.f);
      float qv = 0.f;
#pragma unroll
      for (int i = 0; i < 16; ++i) { const float d = xv[i] - mean; qv = fmaf(d, d, qv); }
      const float var = wave_sum(qv) * (1.f / 1024.f);
      const float mb = bfr(mean), sd = bfr(sqrtf(bfr(bfr(var) + 1e-5f)));
      const int eks = b / 28;
      float x0 = xv[0], x1 = xv[1];
#pragma unroll
      for (int i = 1; i < 8; ++i) { x0 = (eks == i) ? xv[2 * i] : x0; x1 = (eks == i) ? xv[2 * i + 1] : x1; }
      const float h0 = bfr(bfr(bfr(bfr(x0 - mb) / sd) * bfr(1.f + cbv[0])) + sbv[0]);
      const float h1 = bfr(bfr(bfr(bfr(x1 - mb) / sd) * bfr(1.f + cbv[1])) + sbv[1]);
      *(LAS unsigned*)(XB0 + 2 * lane) = (__float_as_uint(h0) >> 16) | (__float_as_uint(h1) & 0xffff0000u);
    }
    cbar();
    STAMP();
    const float rg = gemv_tile(34, XB0);
    const float ru = gemv_tile(36, XB0);
    if (lane < 32) {
      st_gran((gu64*)(ws + WS_GG) + (b / 28) * FF + (b % 28) * 128 + 32 * cw + lane, rg);
      st_gran((gu64*)(ws + WS_GU) + (b / 28) * FF + (b % 28) * 128 + 32 * cw + lane, ru);
    }
  }
  STAMP();
  {
    cbar();
    if (cw < 2) {
      const int c = ct < 112 ? ct : 0;
      const gu64* g = (const gu64*)(ws + WS_GG) + 112 * (b >> 3) + c;
      float v[16];
      GATHER(16, v, g + (k & 7) * FF + (k >> 3) * (8 * FF));
      const float gt = bfr(((v[0] + v[1]) + (v[2] + v[3])) + ((v[4] + v[5]) + (v[6] + v[7])));
      const float up = bfr(((v[8] + v[9]) + (v[10] + v[11])) + ((v[12] + v[13]) + (v[14] + v[15])));
      const float inner = 0.7978845608028654f * (gt + 0.044715f * gt * gt * gt);
      const float ge = bfr(0.5f * gt * (1.f + tanhf(inner)));
      XB0[ct] = ct < 112 ? (u16)(__float_as_uint(bfr(ge * up)) >> 16) : (u16)0;
    }
    cbar();
  }
  STAMP();
  {
    const float r = gemv_tile(QD, XB0);
    if (lane < 32) st_gran((gu64*)(ws + WS_GD) + (b >> 3) * 1024 + (b & 7) * 128 + 32 * cw + lane, r);
  }
  STAMP();
  if (b < 64 && cw == 3) {
    const gu64* g = (const gu64*)(ws + WS_GD) + (size_t)(lane >> 4) * 1024 + 16 * b + (lane & 15);
    float v[8];
    GATHER(8, v, g + k * 4096);
    float s = ((v[0] + v[1]) + (v[2] + v[3])) + ((v[4] + v[5]) + (v[6] + v[7]));
    s = xrow16_sum(s);
    const float o = x1r + bfr(gbv * bfr(s));
    if (lane < 16) a.out[16 * b + lane] = (u16)(__float_as_uint(bfr(o)) >> 16);
  }
  STAMP();
}

extern "C" void kernel_launch(void* const* d_in, const int* in_sizes, int n_in, void* d_out, int out_size, void* d_ws, size_t ws_size,
                              hipStream_t stream) {
  Args a;
  a.x = (const u16*)d_in[0]; a.kc = (const u16*)d_in[1]; a.vc = (const u16*)d_in[2]; a.wq = (const u16*)d_in[3]; a.wo = (const u16*)d_in[4];
  a.wg = (const u16*)d_in[6]; a.wu = (const u16*)d_in[7]; a.wd = (const u16*)d_in[8]; a.c = (const u16*)d_in[9]; a.wmod = (const u16*)d_in[10];
  a.ws = (unsigned char*)d_ws; a.out = (u16*)d_out;
  fused_kernel<<<NWG, NTHR, 0, stream>>>(a);
}
```

```cpp
#include <hip/hip_runtime.h>
#include <hip/hip_bf16.h>
#include <stdint.h>

#define GAS __attribute__((address_space(1)))
#define LAS __attribute__((address_space(3)))
typedef unsigned short u16;
typedef unsigned long long u64;
typedef GAS u64 gu64;
using u32x4 = __attribute__((ext_vector_type(4))) unsigned;
using f32x4 = __attribute__((ext_vector_type(4))) float;
using bf16x8 = __attribute__((ext_vector_type(8))) __bf16;
#define RLX_AGENT __ATOMIC_RELAXED, __HIP_MEMORY_SCOPE_AGENT

#define GOP 1
#define GOSIG(n) do { if (GOP == (n) && lane == 0) __hip_atomic_fetch_add((LAS unsigned*)GO, 1u, __ATOMIC_RELAXED, __HIP_MEMORY_SCOPE_WORKGROUP); } while (0)
#define INFL 2
#define THIN 0
#define STAMPS 0

constexpr int SK = 65536, DH = 128, FF = 3584;
constexpr int NWG = 256, NTHR = 320;
constexpr int PREC = 132;

constexpr size_t WS_EPOCH = 0;
constexpr size_t WS_FLAG0 = 4096;
constexpr size_t WS_FLAGC = 8192;
constexpr size_t WS_GA = 16384;
constexpr size_t WS_GB = WS_GA + 4 * 2048 * 8;
constexpr size_t WS_GQ = WS_GB + 8 * 4096 * 8;
constexpr size_t WS_GW = WS_GQ + 4 * 4096 * 8;
constexpr size_t WS_GX = WS_GW + 32 * 1024 * 8;
constexpr size_t WS_GG = WS_GX + 1024 * 8;
constexpr size_t WS_GU = WS_GG + 8 * FF * 8;
constexpr size_t WS_GD = WS_GU + 8 * FF * 8;
constexpr size_t WS_PART = WS_GD + 32 * 1024 * 8;
constexpr size_t WS_STAMP = WS_PART + (size_t)256 * 8 * PREC * 4;

constexpr int RING = 0, SLOT = 16384, NSLOT = 8;
constexpr int L_FULL = NSLOT * SLOT, L_FREE = L_FULL + 64, L_CBAR = L_FULL + 128, L_DEAD = L_FULL + 132, L_GO = L_FULL + 136, L_CLR = L_FULL + 140;
constexpr int L_RED = L_FULL + 256;
constexpr int L_V1 = L_RED + 2048;
constexpr int L_V2 = L_V1 + 4096;
constexpr int L_MRG = L_V2 + 4096;
constexpr int L_XB = L_MRG + 4 * 8 * PREC * 4;
constexpr int LDS_BYTES = L_XB + 1024;

constexpr long long TCAP = 2000000;

__device__ __forceinline__ float bf2f(u16 v) { return __uint_as_float(((unsigned)v) << 16); }
__device__ __forceinline__ float bfr(float x) { unsigned u = __float_as_uint(x); u = (u + 0x7fffu + ((u >> 16) & 1u)) & 0xffff0000u; return __uint_as_float(u); }
__device__ __forceinline__ float lo16(unsigned w) { return __uint_as_float(w << 16); }
__device__ __forceinline__ float hi16(unsigned w) { return __uint_as_float(w & 0xffff0000u); }
__device__ __forceinline__ unsigned cvtpk(float lo, float hi) {
  typedef __attribute__((ext_vector_type(2))) __bf16 bf16x2_t;
  typedef __attribute__((ext_vector_type(2))) float f32x2_t;
  const f32x2_t v = {lo, hi};
  return __builtin_bit_cast(unsigned, __builtin_convertvector(v, bf16x2_t));
}
__device__ __forceinline__ u64 rt() { return __builtin_amdgcn_s_memrealtime(); }
__device__ __forceinline__ float xrow16_max(float x) {
  auto s = __builtin_amdgcn_permlane16_swap(__float_as_uint(x), __float_as_uint(x), false, false);
  x = fmaxf(__uint_as_float(s[0]), __uint_as_float(s[1]));
  auto t = __builtin_amdgcn_permlane32_swap(__float_as_uint(x), __float_as_uint(x), false, false);
  return fmaxf(__uint_as_float(t[0]), __uint_as_float(t[1]));
}
__device__ __forceinline__ float xrow16_sum(float x) {
  auto s = __builtin_amdgcn_permlane16_swap(__float_as_uint(x), __float_as_uint(x), false, false);
  x = __uint_as_float(s[0]) + __uint_as_float(s[1]);
  auto t = __builtin_amdgcn_permlane32_swap(__float_as_uint(x), __float_as_uint(x), false, false);
  return __uint_as_float(t[0]) + __uint_as_float(t[1]);
}
__device__ __forceinline__ void fma8(float (&acc)[8], float xv, u32x4 w) {
#pragma unroll
  for (int k = 0; k < 4; ++k) { acc[2 * k] = fmaf(xv, lo16(w[k]), acc[2 * k]); acc[2 * k + 1] = fmaf(xv, hi16(w[k]), acc[2 * k + 1]); }
}
template <int CTRL> __device__ __forceinline__ float dpp(float x) {
  return __builtin_bit_cast(float, __builtin_amdgcn_mov_dpp(__builtin_bit_cast(int, x), CTRL, 0xf, 0xf, true));
}
__device__ __forceinline__ float wave_sum(float v) {
  v += dpp<0xB1>(v); v += dpp<0x4E>(v); v += dpp<0x141>(v); v += dpp<0x140>(v);
  return xrow16_sum(v);
}
__device__ __forceinline__ float wave_max(float v) {
  v = fmaxf(v, dpp<0xB1>(v)); v = fmaxf(v, dpp<0x4E>(v)); v = fmaxf(v, dpp<0x141>(v)); v = fmaxf(v, dpp<0x140>(v));
  return xrow16_max(v);
}
__device__ __forceinline__ void st_gran(gu64* g, float v) { __hip_atomic_store(g, (1ull << 32) | (u64)__float_as_uint(v), RLX_AGENT); }
__device__ __forceinline__ void st_u64(gu64* g, u64 v) { __hip_atomic_store(g, v, RLX_AGENT); }
__device__ __forceinline__ u64 ld_u64(const gu64* g) { return __hip_atomic_load(g, RLX_AGENT); }
__device__ __forceinline__ u32x4 ldnt(const void* p) { return __builtin_nontemporal_load(reinterpret_cast<const u32x4*>(p)); }
#define PIN() do { asm volatile("" ::: "memory"); __builtin_amdgcn_sched_barrier(0); } while (0)
#define VM_DRAIN() asm volatile("s_waitcnt vmcnt(0)" ::: "memory")
#define LDS_WAIT() asm volatile("s_waitcnt lgkmcnt(0)" ::: "memory")
#define GLDS(src, dst) __builtin_amdgcn_global_load_lds((const unsigned*)(src), (unsigned*)(dst), 16, 0, 2)

struct Args {
  const u16* x; const u16* kc; const u16* vc; const u16* wq; const u16* wo; const u16* wg; const u16* wu; const u16* wd;
  const u16* c; const u16* wmod; unsigned char* ws; u16* out;
};

__global__ void __launch_bounds__(NTHR, 1) fused_kernel(Args a) {
  __shared__ __attribute__((aligned(16))) unsigned char lds[LDS_BYTES];
  LAS unsigned char* L8 = (LAS unsigned char*)lds;
  volatile LAS unsigned* FULL = (volatile LAS unsigned*)(L8 + L_FULL);
  volatile LAS unsigned* FREE = (volatile LAS unsigned*)(L8 + L_FREE);
  volatile LAS unsigned* CBAR = (volatile LAS unsigned*)(L8 + L_CBAR);
  volatile LAS unsigned* DEAD = (volatile LAS unsigned*)(L8 + L_DEAD);
  volatile LAS unsigned* GO = (volatile LAS unsigned*)(L8 + L_GO);
  volatile LAS unsigned* CLR = (volatile LAS unsigned*)(L8 + L_CLR);
  const int tid = threadIdx.x, lane = tid & 63, wave = __builtin_amdgcn_readfirstlane(tid >> 6), b = blockIdx.x;
  if (tid < 64) ((LAS unsigned*)(L8 + L_FULL))[tid] = 0u;
  __syncthreads();
  const bool ffn = b < 224;
  const int QD = ffn ? 40 : 36, NFILL = QD + 2;
  unsigned char* ws = a.ws;

  if (wave == 0) {
    int koff[4];
#pragma unroll
    for (int j = 0; j < 4; ++j) { const int row = 4 * j + (lane >> 4); koff[j] = row * 256 + (((lane & 15) ^ row) << 4); }
    const unsigned char* kb = (const unsigned char*)a.kc + ((size_t)(b >> 6) * SK + (size_t)(b & 63) * 1024) * 256;
    const unsigned char* vb = (const unsigned char*)a.vc + ((size_t)(b >> 6) * SK + (size_t)(b & 63) * 1024) * 256;
    {
      const int ks = b >> 6, cg = b & 63;
      const u64 T = ld_u64((gu64*)(ws + WS_EPOCH)) + 1ull;
      if (lane < 32) st_u64((gu64*)(ws + WS_GA) + ks * 2048 + cg * 32 + lane, 0);
      st_u64((gu64*)(ws + WS_GQ) + ks * 4096 + cg * 64 + lane, 0);
      if (lane < 4) st_u64((gu64*)(ws + WS_FLAGC) + 4 * b + lane, 0);
#pragma unroll
      for (int i = 0; i < 2; ++i) {
        st_u64((gu64*)(ws + WS_GW) + (b >> 3) * 1024 + (b & 7) * 128 + 64 * i + lane, 0);
        st_u64((gu64*)(ws + WS_GB) + (b >> 5) * 4096 + (b & 31) * 128 + 64 * i + lane, 0);
        st_u64((gu64*)(ws + WS_GD) + (b >> 3) * 1024 + (b & 7) * 128 + 64 * i + lane, 0);
        if (ffn) {
          st_u64((gu64*)(ws + WS_GG) + (b / 28) * FF + (b % 28) * 128 + 64 * i + lane, 0);
          st_u64((gu64*)(ws + WS_GU) + (b / 28) * FF + (b % 28) * 128 + 64 * i + lane, 0);
        }
      }
      if (b < 64 && lane < 8) st_u64((gu64*)(ws + WS_GX) + b * 8 + lane, 0);
      VM_DRAIN();
      *CLR = 1u;
      if (lane == 0) st_u64((gu64*)(ws + WS_FLAG0) + b, T);
    }
    int pub = 0; bool dead = false;
    { int sp = 0; while (*GO < 4u && ++sp < 10000000) __builtin_amdgcn_s_sleep(1); }
    for (int q = 0; q < NFILL; ++q) {
      const int s = q & 7;
      if (q >= NSLOT) {
        const unsigned need = (unsigned)(q - NSLOT + 1);
        if (FREE[s] < need && pub < q) { VM_DRAIN(); for (; pub < q; ++pub) FULL[pub & 7] = (unsigned)(pub + 1); }
        u64 t0 = 0; int sp = 0;
        while (FREE[s] < need) { __builtin_amdgcn_s_sleep(1); if ((++sp & 1023) == 0) { if (*DEAD != 0u) { dead = true; break; } if (t0 == 0) t0 = rt(); else if ((long long)(rt() - t0) > TCAP) { dead = true; break; } } }
        if (dead) { *DEAD = 1u; break; }
        __builtin_amdgcn_fence(__ATOMIC_ACQUIRE, "workgroup");
      }
      unsigned char* dst = lds + s * SLOT;
      if (q < 32) {
        const size_t o = (size_t)q * 8192;
#pragma unroll
        for (int j = 0; j < 8; ++j) GLDS(kb + o + koff[j & 3] + (j >> 2) * 4096, dst + j * 1024);
#pragma unroll
        for (int j = 0; j < 8; ++j) GLDS(vb + o + koff[j & 3] + (j >> 2) * 4096, dst + 8192 + j * 1024);
      } else {
        const unsigned char* src; size_t step; int n = 16;
        if (q < 34) { src = (const unsigned char*)(a.wmod + (size_t)(128 * (b >> 5) + 64 * (q - 32) + (lane >> 4)) * 6144 + 2048 + 128 * (b & 31) + (lane & 15) * 8); step = (size_t)4 * 6144 * 2; }
        else if (q < 36) { src = (const unsigned char*)(a.wo + (size_t)(128 * (b >> 3) + 64 * (q - 34) + (lane >> 4)) * 1024 + 128 * (b & 7) + (lane & 15) * 8); step = (size_t)4 * 1024 * 2; }
        else if (q < QD) { const u16* w = (q < 38) ? a.wg : a.wu; const int hf = (q - 36) & 1;
          src = (const unsigned char*)(w + (size_t)(128 * (b / 28) + 64 * hf + (lane >> 4)) * FF + 128 * (b % 28) + (lane & 15) * 8); step = (size_t)4 * FF * 2; }
        else { src = (const unsigned char*)(a.wd + (size_t)(112 * (b >> 3) + 64 * (q - QD) + (lane >> 4)) * 1024 + 128 * (b & 7) + (lane & 15) * 8); step = (size_t)4 * 1024 * 2; if (q == QD + 1) n = 12; }
#pragma unroll
        for (int j = 0; j < 12; ++j) GLDS(src + j * step, dst + j * 1024);
        if (n == 16) {
#pragma unroll
          for (int j = 12; j < 16; ++j) GLDS(src + j * step, dst + j * 1024);
        }
      }
      if (THIN && *GO < 8u) {
        VM_DRAIN();
        for (; pub <= q; ++pub) FULL[pub & 7] = (unsigned)(pub + 1);
      } else if (q + 1 - pub > INFL && q + 1 < NFILL) {
        if (INFL == 1) asm volatile("s_waitcnt vmcnt(16)" ::: "memory");
        else if (INFL == 2) asm volatile("s_waitcnt vmcnt(32)" ::: "memory");
        else asm volatile("s_waitcnt vmcnt(48)" ::: "memory");
        FULL[pub & 7] = (unsigned)(pub + 1); ++pub;
      }
    }
    VM_DRAIN();
    if (!dead) for (; pub < NFILL; ++pub) FULL[pub & 7] = (unsigned)(pub + 1);
    return;
  }

  const int cw = wave - 1, ct = tid - 64;
  LAS float* RED = (LAS float*)(L8 + L_RED);
  LAS float* V1 = (LAS float*)(L8 + L_V1);
  LAS float* V2 = (LAS float*)(L8 + L_V2);
  LAS float* MRG = (LAS float*)(L8 + L_MRG);
  unsigned cepoch = 0;
#define STAMP() do {} while (0)
  auto spin = [&](u64& t0, int& sp) -> bool {
    if (++sp > 64) __builtin_amdgcn_s_sleep(1);
    if ((sp & 1023) != 0) return false;
    if (*DEAD != 0u) return true;
    if (t0 == 0) { t0 = rt(); return false; }
    if ((long long)(rt() - t0) > TCAP) { *DEAD = 1u; return true; }
    return false;
  };
  auto cbar = [&]() {
    LDS_WAIT();
    cepoch += 4;
    if (lane == 0) __hip_atomic_fetch_add((LAS unsigned*)CBAR, 1u, __ATOMIC_RELAXED, __HIP_MEMORY_SCOPE_WORKGROUP);
    u64 t0 = 0; int sp = 0;
    while (*CBAR < cepoch) { if (spin(t0, sp)) break; }
    asm volatile("" ::: "memory");
  };
  auto wait_full = [&](int q) {
    u64 t0 = 0; int sp = 0;
    while (FULL[q & 7] < (unsigned)(q + 1)) { if (spin(t0, sp)) break; }
    asm volatile("" ::: "memory");
  };
#define GATHER(N, v, ADDR)                                                                      \
  do { u64 t0_ = 0; int sp_ = 0;                                                        \
    for (;;) { bool ok_ = true; u64 x_[N];                                                       \
      _Pragma("unroll") for (int k = 0; k < N; ++k) x_[k] = ld_u64(ADDR);                        \
      _Pragma("unroll") for (int k = 0; k < N; ++k) { v[k] = __uint_as_float((unsigned)x_[k]); ok_ &= (unsigned)(x_[k] >> 32) == 1u; } \
      if (__all(ok_)) break;                                                                     \
      if (spin(t0_, sp_)) break; }                                                               \
  } while (0)

  STAMP();
  const int ks = b >> 6, cg = b & 63;
  const u64 Traw = ld_u64((gu64*)(ws + WS_EPOCH));
  const u32x4 xa = *(const u32x4*)(a.x + lane * 8), xb = *(const u32x4*)(a.x + 512 + lane * 8);
  const u16 xs_raw = a.x[256 * ks + ct];
  u16 cA[4];
  const u16 cBr = a.c[128 * (b >> 5) + (ct & 127)];
#pragma unroll
  for (int i = 0; i < 4; ++i) cA[i] = a.c[256 * ks + 64 * cw + 16 * i + (lane >> 2)];
  PIN();
  u32x4 wA[4], wQ[8];
#pragma unroll
  for (int i = 0; i < 4; ++i) wA[i] = ldnt(a.wmod + (size_t)(256 * ks + 64 * cw + 16 * i + (lane >> 2)) * 6144 + 32 * cg + (lane & 3) * 8);
#pragma unroll
  for (int i = 0; i < 8; ++i) wQ[i] = ldnt(a.wq + (size_t)(256 * ks + 128 * (i >> 2) + 32 * cw + 8 * (i & 3) + (lane >> 3)) * 4096 + 64 * cg + (lane & 7) * 8);
  PIN();
  GOSIG(0);
  const u64 T = Traw + 1ull;
  float mean1, sd1;
  {
    float xv[16];
#pragma unroll
    for (int k = 0; k < 4; ++k) { xv[2 * k] = lo16(xa[k]); xv[2 * k + 1] = hi16(xa[k]); xv[8 + 2 * k] = lo16(xb[k]); xv[9 + 2 * k] = hi16(xb[k]); }
    float s = 0.f;
#pragma unroll
    for (int i = 0; i < 16; ++i) s += xv[i];
    const float mean = wave_sum(s) * (1.f / 1024.f);
    float qv = 0.f;
#pragma unroll
    for (int i = 0; i < 16; ++i) { const float d = xv[i] - mean; qv = fmaf(d, d, qv); }
    const float var = wave_sum(qv) * (1.f / 1024.f);
    mean1 = bfr(mean); sd1 = bfr(sqrtf(bfr(bfr(var) + 1e-5f)));
  }
  const float ln_s = bfr(bfr(bf2f(xs_raw) - mean1) / sd1);
  float sA[4];
#pragma unroll
  for (int i = 0; i < 4; ++i) { const float cv = bf2f(cA[i]); sA[i] = bfr(cv / (1.f + __expf(-cv))); }
  {
    float acc[8] = {0, 0, 0, 0, 0, 0, 0, 0};
#pragma unroll
    for (int i = 0; i < 4; ++i) fma8(acc, sA[i], wA[i]);
#pragma unroll
    for (int j = 0; j < 8; ++j) { acc[j] += dpp<0x124>(acc[j]); acc[j] += dpp<0x128>(acc[j]); acc[j] = xrow16_sum(acc[j]); }
    if (lane < 4) {
#pragma unroll
      for (int j = 0; j < 8; ++j) RED[cw * 128 + lane * 8 + j] = acc[j];
    }
    { u64 t0 = 0; int sp = 0; while (*CLR == 0u) { if (spin(t0, sp)) break; } }
    cbar();
    if (ct < 32) st_gran((gu64*)(ws + WS_GA) + ks * 2048 + cg * 32 + ct, (RED[ct] + RED[128 + ct]) + (RED[256 + ct] + RED[384 + ct]));
  }
  GOSIG(1);
  STAMP();
  {
    const gu64* f = (const gu64*)(ws + WS_FLAG0);
    u64 t0 = 0; int sp = 0;
    for (;;) {
      const u64 v0 = ld_u64(f + lane), v1 = ld_u64(f + 64 + lane), v2 = ld_u64(f + 128 + lane), v3 = ld_u64(f + 192 + lane);
      if (__all(v0 == T && v1 == T && v2 == T && v3 == T)) break;
      if (spin(t0, sp)) break;
    }
    asm volatile("" ::: "memory");
    if (b == 0 && ct == 0) st_u64((gu64*)(ws + WS_EPOCH), T);
  }
  STAMP();
  {
    const gu64* g = (const gu64*)(ws + WS_GA) + 256 * ks + ct;
    float v[8];
    GATHER(8, v, g + (k & 3) * 2048 + (k >> 2) * 1024);
    const float sa = bfr((v[0] + v[1]) + (v[2] + v[3])), ca = bfr((v[4] + v[5]) + (v[6] + v[7]));
    V2[ct] = bfr(bfr(ln_s * bfr(1.f + ca)) + sa);
    cbar();
  }
  GOSIG(2);
  STAMP();
  {
    float aq[8] = {0, 0, 0, 0, 0, 0, 0, 0};
#pragma unroll
    for (int i = 0; i < 8; ++i) fma8(aq, V2[128 * (i >> 2) + 32 * cw + 8 * (i & 3) + (lane >> 3)], wQ[i]);
#pragma unroll
    for (int j = 0; j < 8; ++j) { aq[j] += dpp<0x128>(aq[j]); aq[j] = xrow16_sum(aq[j]); }
    if (lane < 8) {
#pragma unroll
      for (int j = 0; j < 8; ++j) RED[cw * 128 + lane * 8 + j] = aq[j];
    }
    cbar();
    if (ct < 64) st_gran((gu64*)(ws + WS_GQ) + ks * 4096 + cg * 64 + ct, (RED[ct] + RED[128 + ct]) + (RED[256 + ct] + RED[384 + ct]));
  }
  GOSIG(3);
  STAMP();
  const int lr = lane & 15, lg = lane >> 4;
  int loff[4];
#pragma unroll
  for (int s2 = 0; s2 < 4; ++s2) loff[s2] = lr * 256 + (((4 * s2 + lg) ^ lr) << 4);
  u32x4 kf[8], vf[8], kn[8], vn[8];
#define LOADKV(kf, vf, q_)                                                                                 \
  do { wait_full(q_);                                                                              \
    const LAS unsigned char* sb_ = L8 + RING + ((q_) & 7) * SLOT;                                  \
    _Pragma("unroll") for (int t_ = 0; t_ < 2; ++t_) _Pragma("unroll") for (int s_ = 0; s_ < 4; ++s_) { \
      kf[t_ * 4 + s_] = *(const LAS u32x4*)(sb_ + loff[s_] + t_ * 4096);                          \
      vf[t_ * 4 + s_] = *(const LAS u32x4*)(sb_ + loff[s_] + t_ * 4096 + 8192); }                 \
    LDS_WAIT();                                                                                    \
    if (lane == 0) FREE[(q_) & 7] = (unsigned)((q_) + 1);                                          \
    asm volatile("" : "+v"(kf[0]), "+v"(kf[1]), "+v"(kf[2]), "+v"(kf[3]), "+v"(kf[4]), "+v"(kf[5]), "+v"(kf[6]), "+v"(kf[7]), \
                      "+v"(vf[0]), "+v"(vf[1]), "+v"(vf[2]), "+v"(vf[3]), "+v"(vf[4]), "+v"(vf[5]), "+v"(vf[6]), "+v"(vf[7]) : : "memory"); \
  } while (0)
  const bool haveA = __builtin_amdgcn_readfirstlane(FULL[cw & 7]) >= (unsigned)(cw + 1);
  if (haveA) LOADKV(kf, vf, cw);
  const bool haveB = haveA && __builtin_amdgcn_readfirstlane(FULL[(cw + 4) & 7]) >= (unsigned)(cw + 5);
  if (haveB) LOADKV(kn, vn, cw + 4);
  STAMP();
  const int kvh = ks;
  {
    const gu64* g = (const gu64*)(ws + WS_GQ) + 1024 * kvh + ct;
    float v[16];
    GATHER(16, v, g + (k & 3) * 4096 + (k >> 2) * 256);
#pragma unroll
    for (int i = 0; i < 4; ++i) V1[ct + 256 * i] = bfr((v[4 * i] + v[4 * i + 1]) + (v[4 * i + 2] + v[4 * i + 3]));
    cbar();
  }
  if (lane == 0) __hip_atomic_fetch_add((LAS unsigned*)GO, 1u, __ATOMIC_RELAXED, __HIP_MEMORY_SCOPE_WORKGROUP);
  STAMP();
  constexpr float SCALE = 0.08838834764831845f, LOG2E = 1.4426950408889634f;
  {
    const int r = lane & 15, g = lane >> 4;
    u32x4 qf[4];
#pragma unroll
    for (int s = 0; s < 4; ++s) {
      const LAS f32x4* qp = (const LAS f32x4*)(V1 + (r & 7) * DH + s * 32 + g * 8);
      const f32x4 p0 = qp[0], p1 = qp[1];
      u32x4 v = {cvtpk(p0[0], p0[1]), cvtpk(p0[2], p0[3]), cvtpk(p1[0], p1[1]), cvtpk(p1[2], p1[3])};
      if (r >= 8) v = u32x4{0u, 0u, 0u, 0u};
      qf[s] = v;
    }
    u32x4 sel[2];
#pragma unroll
    for (int h = 0; h < 2; ++h) {
      const int kk = r + 16 * h - 8 * g;
#pragma unroll
      for (int w = 0; w < 4; ++w) sel[h][w] = (kk == 2 * w ? 0x3f80u : 0u) | (kk == 2 * w + 1 ? 0x3f800000u : 0u);
    }
    f32x4 acc[8];
#pragma unroll
    for (int i = 0; i < 8; ++i) acc[i] = f32x4{0.f, 0.f, 0.f, 0.f};
    float m = -INFINITY, l = 0.f;
    auto step = [&](const u32x4 (&kf)[8], const u32x4 (&vf)[8]) {
      f32x4 s0 = {0.f, 0.f, 0.f, 0.f}, s1 = {0.f, 0.f, 0.f, 0.f};
#pragma unroll
      for (int s = 0; s < 4; ++s) {
        s0 = __builtin_amdgcn_mfma_f32_16x16x32_bf16(__builtin_bit_cast(bf16x8, kf[s]), __builtin_bit_cast(bf16x8, qf[s]), s0, 0, 0, 0);
        s1 = __builtin_amdgcn_mfma_f32_16x16x32_bf16(__builtin_bit_cast(bf16x8, kf[4 + s]), __builtin_bit_cast(bf16x8, qf[s]), s1, 0, 0, 0);
      }
      float t[8];
#pragma unroll
      for (int j = 0; j < 4; ++j) { t[j] = bfr(bfr(s0[j]) * SCALE); t[4 + j] = bfr(bfr(s1[j]) * SCALE); }
      float mx = t[0];
#pragma unroll
      for (int j = 1; j < 8; ++j) mx = fmaxf(mx, t[j]);
      mx = xrow16_max(mx);
      const float mn = fmaxf(m, mx);
      const float alpha = __builtin_amdgcn_exp2f((m - mn) * LOG2E);
      m = mn;
      float p[8], ps = 0.f;
#pragma unroll
      for (int j = 0; j < 8; ++j) { p[j] = __builtin_amdgcn_exp2f((t[j] - mn) * LOG2E); ps += p[j]; }
      l = fmaf(l, alpha, ps);
      const u32x4 pb = {cvtpk(p[0], p[1]), cvtpk(p[2], p[3]), cvtpk(p[4], p[5]), cvtpk(p[6], p[7])};
#pragma unroll
      for (int i = 0; i < 8; ++i) acc[i] *= alpha;
#pragma unroll
      for (int db = 0; db < 8; ++db) {
        const f32x4 z = {0.f, 0.f, 0.f, 0.f};
        const f32x4 d0 = __builtin_amdgcn_mfma_f32_16x16x32_bf16(__builtin_bit_cast(bf16x8, vf[db >> 1]), __builtin_bit_cast(bf16x8, sel[db & 1]), z, 0, 0, 0);
        const f32x4 d1 = __builtin_amdgcn_mfma_f32_16x16x32_bf16(__builtin_bit_cast(bf16x8, vf[4 + (db >> 1)]), __builtin_bit_cast(bf16x8, sel[db & 1]), z, 0, 0, 0);
        const u32x4 vt = {cvtpk(d0[0], d0[1]), cvtpk(d0[2], d0[3]), cvtpk(d1[0], d1[1]), cvtpk(d1[2], d1[3])};
        acc[db] = __builtin_amdgcn_mfma_f32_16x16x32_bf16(__builtin_bit_cast(bf16x8, vt), __builtin_bit_cast(bf16x8, pb), acc[db], 0, 0, 0);
      }
    };
    if (!haveA) LOADKV(kf, vf, cw);
    step(kf, vf);
    if (!haveB) LOADKV(kn, vn, cw + 4);
    step(kn, vn);
#pragma unroll 1
    for (int st = cw + 8; st < 32; st += 4) { LOADKV(kf, vf, st); step(kf, vf); }
    l = xrow16_sum(l);
    if (r < 8) {
      LAS float* rec = MRG + (cw * 8 + r) * PREC;
      if (g == 0) { rec[0] = m; rec[1] = l; }
#pragma unroll
      for (int db = 0; db < 8; ++db) *(LAS f32x4*)(rec + 4 + db * 16 + g * 4) = acc[db];
    }
    cbar();
    STAMP();
#pragma unroll
    for (int hh = 0; hh < 2; ++hh) {
      const int h = 2 * cw + hh;
      float M = -INFINITY;
#pragma unroll
      for (int w = 0; w < 4; ++w) M = fmaxf(M, MRG[(w * 8 + h) * PREC]);
      float Ls = 0.f, o0 = 0.f, o1 = 0.f;
#pragma unroll
      for (int w = 0; w < 4; ++w) {
        const LAS float* rec = MRG + (w * 8 + h) * PREC;
        const float f = __builtin_amdgcn_exp2f((rec[0] - M) * LOG2E);
        Ls = fmaf(f, rec[1], Ls); o0 = fmaf(f, rec[4 + 2 * lane], o0); o1 = fmaf(f, rec[5 + 2 * lane], o1);
      }
      gu64* out = (gu64*)(ws + WS_PART + ((size_t)b * 8 + h) * PREC * 4);
      if (lane == 0) st_u64(out, ((u64)__float_as_uint(Ls) << 32) | __float_as_uint(M));
      st_u64(out + 2 + lane, ((u64)__float_as_uint(o1) << 32) | __float_as_uint(o0));
    }
    VM_DRAIN();
    if (lane == 0) st_u64((gu64*)(ws + WS_FLAGC) + 4 * b + cw, 1ull);
  }
  STAMP();
  LAS u16* XB0 = (LAS u16*)(L8 + L_XB);
  LAS u16* XBW = XB0 + 128 * cw;
  auto gemv_tile = [&](int q0, const LAS u16* xb) -> float {
    typedef __attribute__((ext_vector_type(4))) short s16x4;
    wait_full(q0); wait_full(q0 + 1);
    f32x4 d0 = {0.f, 0.f, 0.f, 0.f}, d1 = {0.f, 0.f, 0.f, 0.f};
    const int toff = (8 * lg + (lr >> 2)) * 256 + (32 * cw + 4 * (lr & 3)) * 2;
#pragma unroll
    for (int s2 = 0; s2 < 4; ++s2) {
      const u32x4 av = *(const LAS u32x4*)(xb + 32 * s2 + 8 * lg);
      const LAS unsigned char* tb = L8 + RING + ((q0 + (s2 >> 1)) & 7) * SLOT + (s2 & 1) * 8192 + toff;
      const s16x4 p0 = __builtin_amdgcn_ds_read_tr16_b64_v4i16((LAS s16x4*)(tb));
      const s16x4 p1 = __builtin_amdgcn_ds_read_tr16_b64_v4i16((LAS s16x4*)(tb + 1024));
      const s16x4 p2 = __builtin_amdgcn_ds_read_tr16_b64_v4i16((LAS s16x4*)(tb + 32));
      const s16x4 p3 = __builtin_amdgcn_ds_read_tr16_b64_v4i16((LAS s16x4*)(tb + 1056));
      typedef __attribute__((ext_vector_type(8))) short s16x8;
      const s16x8 b0 = __builtin_shufflevector(p0, p1, 0, 1, 2, 3, 4, 5, 6, 7), b1 = __builtin_shufflevector(p2, p3, 0, 1, 2, 3, 4, 5, 6, 7);
      d0 = __builtin_amdgcn_mfma_f32_16x16x32_bf16(__builtin_bit_cast(bf16x8, av), __builtin_bit_cast(bf16x8, b0), d0, 0, 0, 0);
      d1 = __builtin_amdgcn_mfma_f32_16x16x32_bf16(__builtin_bit_cast(bf16x8, av), __builtin_bit_cast(bf16x8, b1), d1, 0, 0, 0);
    }
    return (lane & 16) ? d1[0] : d0[0];
  };
  {
    const float cv = bf2f(cBr);
    if (ct < 128) XB0[ct] = (u16)(__float_as_uint(bfr(cv / (1.f + __expf(-cv)))) >> 16);
    cbar();
    const float r = gemv_tile(32, XB0);
    if (lane < 32) st_gran((gu64*)(ws + WS_GB) + (b >> 5) * 4096 + (b & 31) * 128 + 32 * cw + lane, r);
    cbar();
    if (ct == 0) { FREE[32 & 7] = 33u; FREE[33 & 7] = 34u; }
  }
  {
    const int h = b >> 3, kv = h >> 3;
    const gu64* f = (const gu64*)(ws + WS_FLAGC) + 256 * kv + 4 * lane + ((h & 7) >> 1);
    { u64 t0 = 0; int sp = 0;
      for (;;) { const u64 v = ld_u64(f); if (__all(v == 1ull)) break; if (spin(t0, sp)) break; } }
    asm volatile("" ::: "memory");
    const gu64* pbase = (const gu64*)(ws + WS_PART + ((size_t)kv * 64 * 8 + (h & 7)) * PREC * 4);
    const u64 ml = ld_u64(pbase + (size_t)lane * 4 * PREC);
    u64 ov[16];
#pragma unroll
    for (int i = 0; i < 16; ++i) ov[i] = ld_u64(pbase + (size_t)(16 * lg + i) * 4 * PREC + 2 + 16 * cw + lr);
    const float Ml = __uint_as_float((unsigned)ml), Ll = __uint_as_float((unsigned)(ml >> 32));
    const float M = wave_max(Ml);
    const float f1 = __builtin_amdgcn_exp2f((Ml - M) * LOG2E);
    const float Lt = wave_sum(f1 * Ll);
    const float wgt = f1 / Lt;
    float o0 = 0.f, o1 = 0.f;
#pragma unroll
    for (int i = 0; i < 16; ++i) {
      const float wv = __shfl(wgt, 16 * lg + i);
      o0 = fmaf(wv, __uint_as_float((unsigned)ov[i]), o0); o1 = fmaf(wv, __uint_as_float((unsigned)(ov[i] >> 32)), o1);
    }
    o0 = xrow16_sum(o0); o1 = xrow16_sum(o1);
    if (lane < 16) *(LAS unsigned*)(XB0 + 32 * cw + 2 * lane) = (__float_as_uint(bfr(o0)) >> 16) | (__float_as_uint(bfr(o1)) & 0xffff0000u);
    cbar();
  }
  STAMP();
  {
    const float r = gemv_tile(34, XB0);
    if (lane < 32) st_gran((gu64*)(ws + WS_GW) + (b >> 3) * 1024 + (b & 7) * 128 + 32 * cw + lane, r);
  }
  STAMP();
  float sbv[2] = {0.f, 0.f}, cbv[2] = {0.f, 0.f}, gav = 0.f, gbv = 0.f;
  if (ffn && cw == 0) {
    const gu64* g = (const gu64*)(ws + WS_GB) + 1024 + 128 * (b / 28) + 2 * lane;
#pragma unroll
    for (int w = 0; w < 2; ++w) {
      float v[16];
      GATHER(16, v, g + (k & 7) * 4096 + (k >> 3) + w * 1024);
      const float r0 = bfr(((v[0] + v[1]) + (v[2] + v[3])) + ((v[4] + v[5]) + (v[6] + v[7])));
      const float r1 = bfr(((v[8] + v[9]) + (v[10] + v[11])) + ((v[12] + v[13]) + (v[14] + v[15])));
      if (w == 0) { sbv[0] = r0; sbv[1] = r1; } else { cbv[0] = r0; cbv[1] = r1; }
    }
  }
  if (b < 64 && cw == 3) {
    const gu64* g = (const gu64*)(ws + WS_GB) + 16 * b + (lane & 15);
    float v[16];
    GATHER(16, v, g + (k & 7) * 4096 + (k >> 3) * 3072);
    gav = bfr(((v[0] + v[1]) + (v[2] + v[3])) + ((v[4] + v[5]) + (v[6] + v[7])));
    gbv = bfr(((v[8] + v[9]) + (v[10] + v[11])) + ((v[12] + v[13]) + (v[14] + v[15])));
  }
  float x1r = 0.f;
  if (b < 64 && cw == 3) {
    const gu64* g = (const gu64*)(ws + WS_GW) + (size_t)(lane >> 4) * 1024 + 16 * b + (lane & 15);
    float v[8];
    GATHER(8, v, g + k * 4096);
    float s = ((v[0] + v[1]) + (v[2] + v[3])) + ((v[4] + v[5]) + (v[6] + v[7]));
    s = xrow16_sum(s);
    x1r = bfr(bf2f(a.x[16 * b + (lane & 15)]) + bfr(gav * bfr(s)));
    { const float nb = __shfl_down(x1r, 1);
      if (lane < 16 && !(lane & 1)) st_u64((gu64*)(ws + WS_GX) + 8 * b + (lane >> 1), (1ull << 32) | (u64)((__float_as_uint(x1r) >> 16) | (__float_as_uint(nb) & 0xffff0000u))); }
  }
  if (ffn) {
    cbar();
    if (cw == 0) {
      const gu64* g = (const gu64*)(ws + WS_GX) + lane;
      float v[8];
      GATHER(8, v, g + k * 64);
      float xv[16];
#pragma unroll
      for (int i = 0; i < 8; ++i) { const unsigned u = __float_as_uint(v[i]); xv[2 * i] = lo16(u); xv[2 * i + 1] = hi16(u); }
      float s = 0.f;
#pragma unroll
      for (int i = 0; i < 16; ++i) s += xv[i];
      const float mean = wave_sum(s) * (1.f / 1024.f);
      float qv = 0.f;
#pragma unroll
      for (int i = 0; i < 16; ++i) { const float d = xv[i] - mean; qv = fmaf(d, d, qv); }
      const float var = wave_sum(qv) * (1.f / 1024.f);
      const float mb = bfr(mean), sd = bfr(sqrtf(bfr(bfr(var) + 1e-5f)));
      const int eks = b / 28;
      float x0 = xv[0], x1 = xv[1];
#pragma unroll
      for (int i = 1; i < 8; ++i) { x0 = (eks == i) ? xv[2 * i] : x0; x1 = (eks == i) ? xv[2 * i + 1] : x1; }
      const float h0 = bfr(bfr(bfr(bfr(x0 - mb) / sd) * bfr(1.f + cbv[0])) + sbv[0]);
      const float h1 = bfr(bfr(bfr(bfr(x1 - mb) / sd) * bfr(1.f + cbv[1])) + sbv[1]);
      *(LAS unsigned*)(XB0 + 2 * lane) = (__float_as_uint(h0) >> 16) | (__float_as_uint(h1) & 0xffff0000u);
    }
    cbar();
    STAMP();
    const float rg = gemv_tile(36, XB0);
    const float ru = gemv_tile(38, XB0);
    if (lane < 32) {
      st_gran((gu64*)(ws + WS_GG) + (b / 28) * FF + (b % 28) * 128 + 32 * cw + lane, rg);
      st_gran((gu64*)(ws + WS_GU) + (b / 28) * FF + (b % 28) * 128 + 32 * cw + lane, ru);
    }
  }
  STAMP();
  {
    cbar();
    if (cw < 2) {
      const int c = ct < 112 ? ct : 0;
      const gu64* g = (const gu64*)(ws + WS_GG) + 112 * (b >> 3) + c;
      float v[16];
      GATHER(16, v, g + (k & 7) * FF + (k >> 3) * (8 * FF));
      const float gt = bfr(((v[0] + v[1]) + (v[2] + v[3])) + ((v[4] + v[5]) + (v[6] + v[7])));
      const float up = bfr(((v[8] + v[9]) + (v[10] + v[11])) + ((v[12] + v[13]) + (v[14] + v[15])));
      const float inner = 0.7978845608028654f * (gt + 0.044715f * gt * gt * gt);
      const float ge = bfr(0.5f * gt * (1.f + tanhf(inner)));
      XB0[ct] = ct < 112 ? (u16)(__float_as_uint(bfr(ge * up)) >> 16) : (u16)0;
    }
    cbar();
  }
  STAMP();
  {
    const float r = gemv_tile(QD, XB0);
    if (lane < 32) st_gran((gu64*)(ws + WS_GD) + (b >> 3) * 1024 + (b & 7) * 128 + 32 * cw + lane, r);
  }
  STAMP();
  if (b < 64 && cw == 3) {
    const gu64* g = (const gu64*)(ws + WS_GD) + (size_t)(lane >> 4) * 1024 + 16 * b + (lane & 15);
    float v[8];
    GATHER(8, v, g + k * 4096);
    float s = ((v[0] + v[1]) + (v[2] + v[3])) + ((v[4] + v[5]) + (v[6] + v[7]));
    s = xrow16_sum(s);
    const float o = x1r + bfr(gbv * bfr(s));
    if (lane < 16) a.out[16 * b + lane] = (u16)(__float_as_uint(bfr(o)) >> 16);
  }
  STAMP();
}

extern "C" void kernel_launch(void* const* d_in, const int* in_sizes, int n_in, void* d_out, int out_size, void* d_ws, size_t ws_size,
                              hipStream_t stream) {
  Args a;
  a.x = (const u16*)d_in[0]; a.kc = (const u16*)d_in[1]; a.vc = (const u16*)d_in[2]; a.wq = (const u16*)d_in[3]; a.wo = (const u16*)d_in[4];
  a.wg = (const u16*)d_in[6]; a.wu = (const u16*)d_in[7]; a.wd = (const u16*)d_in[8]; a.c = (const u16*)d_in[9]; a.wmod = (const u16*)d_in[10];
  a.ws = (unsigned char*)d_ws; a.out = (u16*)d_out;
  fused_kernel<<<NWG, NTHR, 0, stream>>>(a);
}
```

```cpp
#include <hip/hip_runtime.h>
#include <hip/hip_bf16.h>
#include <stdint.h>

#define GAS __attribute__((address_space(1)))
#define LAS __attribute__((address_space(3)))
typedef unsigned short u16;
typedef unsigned long long u64;
typedef GAS u64 gu64;
using u32x4 = __attribute__((ext_vector_type(4))) unsigned;
using f32x4 = __attribute__((ext_vector_type(4))) float;
using bf16x8 = __attribute__((ext_vector_type(8))) __bf16;
#define RLX_AGENT __ATOMIC_RELAXED, __HIP_MEMORY_SCOPE_AGENT


constexpr int SK = 65536, DH = 128, FF = 3584;
constexpr int NWG = 256, NTHR = 320;
constexpr int PREC = 132;

constexpr size_t WS_EPOCH = 0;
constexpr size_t WS_FLAG0 = 4096;
constexpr size_t WS_FLAGC = 8192;
constexpr size_t WS_GA = 16384;
constexpr size_t WS_GB = WS_GA + 4 * 2048 * 8;
constexpr size_t WS_GQ = WS_GB + 8 * 4096 * 8;
constexpr size_t WS_GW = WS_GQ + 4 * 4096 * 8;
constexpr size_t WS_GX = WS_GW + 32 * 1024 * 8;
constexpr size_t WS_GG = WS_GX + 1024 * 8;
constexpr size_t WS_GU = WS_GG + 8 * FF * 8;
constexpr size_t WS_GD = WS_GU + 8 * FF * 8;
constexpr size_t WS_PART = WS_GD + 32 * 1024 * 8;

constexpr int RING = 0, SLOT = 16384, NSLOT = 8;
constexpr int L_FULL = NSLOT * SLOT, L_FREE = L_FULL + 64, L_CBAR = L_FULL + 128, L_DEAD = L_FULL + 132, L_GO = L_FULL + 136, L_CLR = L_FULL + 140;
constexpr int L_RED = L_FULL + 256;
constexpr int L_V1 = L_RED + 2048;
constexpr int L_V2 = L_V1 + 4096;
constexpr int L_MRG = L_V2 + 4096;
constexpr int L_XB = L_MRG + 4 * 8 * PREC * 4;
constexpr int LDS_BYTES = L_XB + 1024;

constexpr long long TCAP = 2000000;

__device__ __forceinline__ float bf2f(u16 v) { return __uint_as_float(((unsigned)v) << 16); }
__device__ __forceinline__ float bfr(float x) { unsigned u = __float_as_uint(x); u = (u + 0x7fffu + ((u >> 16) & 1u)) & 0xffff0000u; return __uint_as_float(u); }
__device__ __forceinline__ float lo16(unsigned w) { return __uint_as_float(w << 16); }
__device__ __forceinline__ float hi16(unsigned w) { return __uint_as_float(w & 0xffff0000u); }
__device__ __forceinline__ unsigned cvtpk(float lo, float hi) {
  typedef __attribute__((ext_vector_type(2))) __bf16 bf16x2_t;
  typedef __attribute__((ext_vector_type(2))) float f32x2_t;
  const f32x2_t v = {lo, hi};
  return __builtin_bit_cast(unsigned, __builtin_convertvector(v, bf16x2_t));
}
__device__ __forceinline__ u64 rt() { return __builtin_amdgcn_s_memrealtime(); }
__device__ __forceinline__ float xrow16_max(float x) {
  auto s = __builtin_amdgcn_permlane16_swap(__float_as_uint(x), __float_as_uint(x), false, false);
  x = fmaxf(__uint_as_float(s[0]), __uint_as_float(s[1]));
  auto t = __builtin_amdgcn_permlane32_swap(__float_as_uint(x), __float_as_uint(x), false, false);
  return fmaxf(__uint_as_float(t[0]), __uint_as_float(t[1]));
}
__device__ __forceinline__ float xrow16_sum(float x) {
  auto s = __builtin_amdgcn_permlane16_swap(__float_as_uint(x), __float_as_uint(x), false, false);
  x = __uint_as_float(s[0]) + __uint_as_float(s[1]);
  auto t = __builtin_amdgcn_permlane32_swap(__float_as_uint(x), __float_as_uint(x), false, false);
  return __uint_as_float(t[0]) + __uint_as_float(t[1]);
}
__device__ __forceinline__ void fma8(float (&acc)[8], float xv, u32x4 w) {
#pragma unroll
  for (int k = 0; k < 4; ++k) { acc[2 * k] = fmaf(xv, lo16(w[k]), acc[2 * k]); acc[2 * k + 1] = fmaf(xv, hi16(w[k]), acc[2 * k + 1]); }
}
template <int CTRL> __device__ __forceinline__ float dpp(float x) {
  return __builtin_bit_cast(float, __builtin_amdgcn_mov_dpp(__builtin_bit_cast(int, x), CTRL, 0xf, 0xf, true));
}
__device__ __forceinline__ float wave_sum(float v) {
  v += dpp<0xB1>(v); v += dpp<0x4E>(v); v += dpp<0x141>(v); v += dpp<0x140>(v);
  return xrow16_sum(v);
}
__device__ __forceinline__ float wave_max(float v) {
  v = fmaxf(v, dpp<0xB1>(v)); v = fmaxf(v, dpp<0x4E>(v)); v = fmaxf(v, dpp<0x141>(v)); v = fmaxf(v, dpp<0x140>(v));
  return xrow16_max(v);
}
__device__ __forceinline__ void st_gran(gu64* g, float v) { __hip_atomic_store(g, (1ull << 32) | (u64)__float_as_uint(v), RLX_AGENT); }
__device__ __forceinline__ void st_u64(gu64* g, u64 v) { __hip_atomic_store(g, v, RLX_AGENT); }
__device__ __forceinline__ u64 ld_u64(const gu64* g) { return __hip_atomic_load(g, RLX_AGENT); }
__device__ __forceinline__ u32x4 ldnt(const void* p) { return __builtin_nontemporal_load(reinterpret_cast<const u32x4*>(p)); }
#define PIN() do { asm volatile("" ::: "memory"); __builtin_amdgcn_sched_barrier(0); } while (0)
#define VM_DRAIN() asm volatile("s_waitcnt vmcnt(0)" ::: "memory")
#define LDS_WAIT() asm volatile("s_waitcnt lgkmcnt(0)" ::: "memory")
#define GLDS(src, dst) __builtin_amdgcn_global_load_lds((const unsigned*)(src), (unsigned*)(dst), 16, 0, 2)

struct Args {
  const u16* x; const u16* kc; const u16* vc; const u16* wq; const u16* wo; const u16* wg; const u16* wu; const u16* wd;
  const u16* c; const u16* wmod; unsigned char* ws; u16* out;
};

__global__ void __launch_bounds__(NTHR, 1) fused_kernel(Args a) {
  __shared__ __attribute__((aligned(16))) unsigned char lds[LDS_BYTES];
  LAS unsigned char* L8 = (LAS unsigned char*)lds;
  volatile LAS unsigned* FULL = (volatile LAS unsigned*)(L8 + L_FULL);
  volatile LAS unsigned* FREE = (volatile LAS unsigned*)(L8 + L_FREE);
  volatile LAS unsigned* CBAR = (volatile LAS unsigned*)(L8 + L_CBAR);
  volatile LAS unsigned* DEAD = (volatile LAS unsigned*)(L8 + L_DEAD);
  volatile LAS unsigned* GO = (volatile LAS unsigned*)(L8 + L_GO);
  volatile LAS unsigned* CLR = (volatile LAS unsigned*)(L8 + L_CLR);
  const int tid = threadIdx.x, lane = tid & 63, wave = __builtin_amdgcn_readfirstlane(tid >> 6), b = blockIdx.x;
  if (tid < 64) ((LAS unsigned*)(L8 + L_FULL))[tid] = 0u;
  __syncthreads();
  const bool ffn = b < 224;
  const int QD = ffn ? 40 : 36, NFILL = QD + 2;
  unsigned char* ws = a.ws;

  if (wave == 0) {
    int koff[4];
#pragma unroll
    for (int j = 0; j < 4; ++j) { const int row = 4 * j + (lane >> 4); koff[j] = row * 256 + (((lane & 15) ^ row) << 4); }
    const unsigned char* kb = (const unsigned char*)a.kc + ((size_t)(b >> 6) * SK + (size_t)(b & 63) * 1024) * 256;
    const unsigned char* vb = (const unsigned char*)a.vc + ((size_t)(b >> 6) * SK + (size_t)(b & 63) * 1024) * 256;
    {
      const int ks = b >> 6, cg = b & 63;
      const u64 T = ld_u64((gu64*)(ws + WS_EPOCH)) + 1ull;
      if (lane < 32) st_u64((gu64*)(ws + WS_GA) + ks * 2048 + cg * 32 + lane, 0);
      st_u64((gu64*)(ws + WS_GQ) + ks * 4096 + cg * 64 + lane, 0);
      if (lane < 4) st_u64((gu64*)(ws + WS_FLAGC) + 4 * b + lane, 0);
#pragma unroll
      for (int i = 0; i < 2; ++i) {
        st_u64((gu64*)(ws + WS_GW) + (b >> 3) * 1024 + (b & 7) * 128 + 64 * i + lane, 0);
        st_u64((gu64*)(ws + WS_GB) + (b >> 5) * 4096 + (b & 31) * 128 + 64 * i + lane, 0);
        st_u64((gu64*)(ws + WS_GD) + (b >> 3) * 1024 + (b & 7) * 128 + 64 * i + lane, 0);
        if (ffn) {
          st_u64((gu64*)(ws + WS_GG) + (b / 28) * FF + (b % 28) * 128 + 64 * i + lane, 0);
          st_u64((gu64*)(ws + WS_GU) + (b / 28) * FF + (b % 28) * 128 + 64 * i + lane, 0);
        }
      }
      if (b < 64 && lane < 8) st_u64((gu64*)(ws + WS_GX) + b * 8 + lane, 0);
      VM_DRAIN();
      *CLR = 1u;
      if (lane == 0) st_u64((gu64*)(ws + WS_FLAG0) + b, T);
    }
    int pub = 0; bool dead = false;
    { int sp = 0; while (*GO < 4u && ++sp < 10000000) __builtin_amdgcn_s_sleep(1); }
    for (int q = 0; q < NFILL; ++q) {
      const int s = q & 7;
      if (q >= NSLOT) {
        const unsigned need = (unsigned)(q - NSLOT + 1);
        if (FREE[s] < need && pub < q) { VM_DRAIN(); for (; pub < q; ++pub) FULL[pub & 7] = (unsigned)(pub + 1); }
        u64 t0 = 0; int sp = 0;
        while (FREE[s] < need) { __builtin_amdgcn_s_sleep(1); if ((++sp & 1023) == 0) { if (*DEAD != 0u) { dead = true; break; } if (t0 == 0) t0 = rt(); else if ((long long)(rt() - t0) > TCAP) { dead = true; break; } } }
        if (dead) { *DEAD = 1u; break; }
        __builtin_amdgcn_fence(__ATOMIC_ACQUIRE, "workgroup");
      }
      unsigned char* dst = lds + s * SLOT;
      if (q < 32) {
        const size_t o = (size_t)q * 8192;
#pragma unroll
        for (int j = 0; j < 8; ++j) GLDS(kb + o + koff[j & 3] + (j >> 2) * 4096, dst + j * 1024);
#pragma unroll
        for (int j = 0; j < 8; ++j) GLDS(vb + o + koff[j & 3] + (j >> 2) * 4096, dst + 8192 + j * 1024);
      } else {
        const unsigned char* src; size_t step; int n = 16;
        if (q < 34) { src = (const unsigned char*)(a.wmod + (size_t)(128 * (b >> 5) + 64 * (q - 32) + (lane >> 4)) * 6144 + 2048 + 128 * (b & 31) + (lane & 15) * 8); step = (size_t)4 * 6144 * 2; }
        else if (q < 36) { src = (const unsigned char*)(a.wo + (size_t)(128 * (b >> 3) + 64 * (q - 34) + (lane >> 4)) * 1024 + 128 * (b & 7) + (lane & 15) * 8); step = (size_t)4 * 1024 * 2; }
        else if (q < QD) { const u16* w = (q < 38) ? a.wg : a.wu; const int hf = (q - 36) & 1;
          src = (const unsigned char*)(w + (size_t)(128 * (b / 28) + 64 * hf + (lane >> 4)) * FF + 128 * (b % 28) + (lane & 15) * 8); step = (size_t)4 * FF * 2; }
        else { src = (const unsigned char*)(a.wd + (size_t)(112 * (b >> 3) + 64 * (q - QD) + (lane >> 4)) * 1024 + 128 * (b & 7) + (lane & 15) * 8); step = (size_t)4 * 1024 * 2; if (q == QD + 1) n = 12; }
#pragma unroll
        for (int j = 0; j < 12; ++j) GLDS(src + j * step, dst + j * 1024);
        if (n == 16) {
#pragma unroll
          for (int j = 12; j < 16; ++j) GLDS(src + j * step, dst + j * 1024);
        }
      }
      if (q + 1 - pub > 2 && q + 1 < NFILL) {
        asm volatile("s_waitcnt vmcnt(32)" ::: "memory");
        FULL[pub & 7] = (unsigned)(pub + 1); ++pub;
      }
    }
    VM_DRAIN();
    if (!dead) for (; pub < NFILL; ++pub) FULL[pub & 7] = (unsigned)(pub + 1);
    return;
  }

  const int cw = wave - 1, ct = tid - 64;
  LAS float* RED = (LAS float*)(L8 + L_RED);
  LAS float* V1 = (LAS float*)(L8 + L_V1);
  LAS float* V2 = (LAS float*)(L8 + L_V2);
  LAS float* MRG = (LAS float*)(L8 + L_MRG);
  unsigned cepoch = 0;
  auto spin = [&](u64& t0, int& sp) -> bool {
    if (++sp > 64) __builtin_amdgcn_s_sleep(1);
    if ((sp & 1023) != 0) return false;
    if (*DEAD != 0u) return true;
    if (t0 == 0) { t0 = rt(); return false; }
    if ((long long)(rt() - t0) > TCAP) { *DEAD = 1u; return true; }
    return false;
  };
  auto cbar = [&]() {
    LDS_WAIT();
    cepoch += 4;
    if (lane == 0) __hip_atomic_fetch_add((LAS unsigned*)CBAR, 1u, __ATOMIC_RELAXED, __HIP_MEMORY_SCOPE_WORKGROUP);
    u64 t0 = 0; int sp = 0;
    while (*CBAR < cepoch) { if (spin(t0, sp)) break; }
    asm volatile("" ::: "memory");
  };
  auto wait_full = [&](int q) {
    u64 t0 = 0; int sp = 0;
    while (FULL[q & 7] < (unsigned)(q + 1)) { if (spin(t0, sp)) break; }
    asm volatile("" ::: "memory");
  };
#define GATHER(N, v, ADDR)                                                                      \
  do { u64 t0_ = 0; int sp_ = 0;                                                        \
    for (;;) { bool ok_ = true; u64 x_[N];                                                       \
      _Pragma("unroll") for (int k = 0; k < N; ++k) x_[k] = ld_u64(ADDR);                        \
      _Pragma("unroll") for (int k = 0; k < N; ++k) { v[k] = __uint_as_float((unsigned)x_[k]); ok_ &= (unsigned)(x_[k] >> 32) == 1u; } \
      if (__all(ok_)) break;                                                                     \
      if (spin(t0_, sp_)) break; }                                                               \
  } while (0)
  const int ks = b >> 6, cg = b & 63;
  const u64 Traw = ld_u64((gu64*)(ws + WS_EPOCH));
  const u32x4 xa = *(const u32x4*)(a.x + lane * 8), xb = *(const u32x4*)(a.x + 512 + lane * 8);
  const u16 xs_raw = a.x[256 * ks + ct];
  u16 cA[4];
  const u16 cBr = a.c[128 * (b >> 5) + (ct & 127)];
#pragma unroll
  for (int i = 0; i < 4; ++i) cA[i] = a.c[256 * ks + 64 * cw + 16 * i + (lane >> 2)];
  PIN();
  u32x4 wA[4], wQ[8];
#pragma unroll
  for (int i = 0; i < 4; ++i) wA[i] = ldnt(a.wmod + (size_t)(256 * ks + 64 * cw + 16 * i + (lane >> 2)) * 6144 + 32 * cg + (lane & 3) * 8);
#pragma unroll
  for (int i = 0; i < 8; ++i) wQ[i] = ldnt(a.wq + (size_t)(256 * ks + 128 * (i >> 2) + 32 * cw + 8 * (i & 3) + (lane >> 3)) * 4096 + 64 * cg + (lane & 7) * 8);
  PIN();
  const u64 T = Traw + 1ull;
  float mean1, sd1;
  {
    float xv[16];
#pragma unroll
    for (int k = 0; k < 4; ++k) { xv[2 * k] = lo16(xa[k]); xv[2 * k + 1] = hi16(xa[k]); xv[8 + 2 * k] = lo16(xb[k]); xv[9 + 2 * k] = hi16(xb[k]); }
    float s = 0.f;
#pragma unroll
    for (int i = 0; i < 16; ++i) s += xv[i];
    const float mean = wave_sum(s) * (1.f / 1024.f);
    float qv = 0.f;
#pragma unroll
    for (int i = 0; i < 16; ++i) { const float d = xv[i] - mean; qv = fmaf(d, d, qv); }
    const float var = wave_sum(qv) * (1.f / 1024.f);
    mean1 = bfr(mean); sd1 = bfr(sqrtf(bfr(bfr(var) + 1e-5f)));
  }
  const float ln_s = bfr(bfr(bf2f(xs_raw) - mean1) / sd1);
  float sA[4];
#pragma unroll
  for (int i = 0; i < 4; ++i) { const float cv = bf2f(cA[i]); sA[i] = bfr(cv / (1.f + __expf(-cv))); }
  {
    float acc[8] = {0, 0, 0, 0, 0, 0, 0, 0};
#pragma unroll
    for (int i = 0; i < 4; ++i) fma8(acc, sA[i], wA[i]);
#pragma unroll
    for (int j = 0; j < 8; ++j) { acc[j] += dpp<0x124>(acc[j]); acc[j] += dpp<0x128>(acc[j]); acc[j] = xrow16_sum(acc[j]); }
    if (lane < 4) {
#pragma unroll
      for (int j = 0; j < 8; ++j) RED[cw * 128 + lane * 8 + j] = acc[j];
    }
    { u64 t0 = 0; int sp = 0; while (*CLR == 0u) { if (spin(t0, sp)) break; } }
    cbar();
    if (ct < 32) st_gran((gu64*)(ws + WS_GA) + ks * 2048 + cg * 32 + ct, (RED[ct] + RED[128 + ct]) + (RED[256 + ct] + RED[384 + ct]));
  }
  if (lane == 0) __hip_atomic_fetch_add((LAS unsigned*)GO, 1u, __ATOMIC_RELAXED, __HIP_MEMORY_SCOPE_WORKGROUP);
  {
    const gu64* f = (const gu64*)(ws + WS_FLAG0);
    u64 t0 = 0; int sp = 0;
    for (;;) {
      const u64 v0 = ld_u64(f + lane), v1 = ld_u64(f + 64 + lane), v2 = ld_u64(f + 128 + lane), v3 = ld_u64(f + 192 + lane);
      if (__all(v0 == T && v1 == T && v2 == T && v3 == T)) break;
      if (spin(t0, sp)) break;
    }
    asm volatile("" ::: "memory");
    if (b == 0 && ct == 0) st_u64((gu64*)(ws + WS_EPOCH), T);
  }
  {
    const gu64* g = (const gu64*)(ws + WS_GA) + 256 * ks + ct;
    float v[8];
    GATHER(8, v, g + (k & 3) * 2048 + (k >> 2) * 1024);
    const float sa = bfr((v[0] + v[1]) + (v[2] + v[3])), ca = bfr((v[4] + v[5]) + (v[6] + v[7]));
    V2[ct] = bfr(bfr(ln_s * bfr(1.f + ca)) + sa);
    cbar();
  }
  {
    float aq[8] = {0, 0, 0, 0, 0, 0, 0, 0};
#pragma unroll
    for (int i = 0; i < 8; ++i) fma8(aq, V2[128 * (i >> 2) + 32 * cw + 8 * (i & 3) + (lane >> 3)], wQ[i]);
#pragma unroll
    for (int j = 0; j < 8; ++j) { aq[j] += dpp<0x128>(aq[j]); aq[j] = xrow16_sum(aq[j]); }
    if (lane < 8) {
#pragma unroll
      for (int j = 0; j < 8; ++j) RED[cw * 128 + lane * 8 + j] = aq[j];
    }
    cbar();
    if (ct < 64) st_gran((gu64*)(ws + WS_GQ) + ks * 4096 + cg * 64 + ct, (RED[ct] + RED[128 + ct]) + (RED[256 + ct] + RED[384 + ct]));
  }
  const int lr = lane & 15, lg = lane >> 4;
  int loff[4];
#pragma unroll
  for (int s2 = 0; s2 < 4; ++s2) loff[s2] = lr * 256 + (((4 * s2 + lg) ^ lr) << 4);
  u32x4 kf[8], vf[8], kn[8], vn[8];
#define LOADKV(kf, vf, q_)                                                                                 \
  do { wait_full(q_);                                                                              \
    const LAS unsigned char* sb_ = L8 + RING + ((q_) & 7) * SLOT;                                  \
    _Pragma("unroll") for (int t_ = 0; t_ < 2; ++t_) _Pragma("unroll") for (int s_ = 0; s_ < 4; ++s_) { \
      kf[t_ * 4 + s_] = *(const LAS u32x4*)(sb_ + loff[s_] + t_ * 4096);                          \
      vf[t_ * 4 + s_] = *(const LAS u32x4*)(sb_ + loff[s_] + t_ * 4096 + 8192); }                 \
    LDS_WAIT();                                                                                    \
    if (lane == 0) FREE[(q_) & 7] = (unsigned)((q_) + 1);                                          \
    asm volatile("" : "+v"(kf[0]), "+v"(kf[1]), "+v"(kf[2]), "+v"(kf[3]), "+v"(kf[4]), "+v"(kf[5]), "+v"(kf[6]), "+v"(kf[7]), \
                      "+v"(vf[0]), "+v"(vf[1]), "+v"(vf[2]), "+v"(vf[3]), "+v"(vf[4]), "+v"(vf[5]), "+v"(vf[6]), "+v"(vf[7]) : : "memory"); \
  } while (0)
  const bool haveA = __builtin_amdgcn_readfirstlane(FULL[cw & 7]) >= (unsigned)(cw + 1);
  if (haveA) LOADKV(kf, vf, cw);
  const bool haveB = haveA && __builtin_amdgcn_readfirstlane(FULL[(cw + 4) & 7]) >= (unsigned)(cw + 5);
  if (haveB) LOADKV(kn, vn, cw + 4);
  const int kvh = ks;
  {
    const gu64* g = (const gu64*)(ws + WS_GQ) + 1024 * kvh + ct;
    float v[16];
    GATHER(16, v, g + (k & 3) * 4096 + (k >> 2) * 256);
#pragma unroll
    for (int i = 0; i < 4; ++i) V1[ct + 256 * i] = bfr((v[4 * i] + v[4 * i + 1]) + (v[4 * i + 2] + v[4 * i + 3]));
    cbar();
  }
  constexpr float SCALE = 0.08838834764831845f, LOG2E = 1.4426950408889634f;
  {
    const int r = lane & 15, g = lane >> 4;
    u32x4 qf[4];
#pragma unroll
    for (int s = 0; s < 4; ++s) {
      const LAS f32x4* qp = (const LAS f32x4*)(V1 + (r & 7) * DH + s * 32 + g * 8);
      const f32x4 p0 = qp[0], p1 = qp[1];
      u32x4 v = {cvtpk(p0[0], p0[1]), cvtpk(p0[2], p0[3]), cvtpk(p1[0], p1[1]), cvtpk(p1[2], p1[3])};
      if (r >= 8) v = u32x4{0u, 0u, 0u, 0u};
      qf[s] = v;
    }
    u32x4 sel[2];
#pragma unroll
    for (int h = 0; h < 2; ++h) {
      const int kk = r + 16 * h - 8 * g;
#pragma unroll
      for (int w = 0; w < 4; ++w) sel[h][w] = (kk == 2 * w ? 0x3f80u : 0u) | (kk == 2 * w + 1 ? 0x3f800000u : 0u);
    }
    f32x4 acc[8];
#pragma unroll
    for (int i = 0; i < 8; ++i) acc[i] = f32x4{0.f, 0.f, 0.f, 0.f};
    float m = -INFINITY, l = 0.f;
    auto step = [&](const u32x4 (&kf)[8], const u32x4 (&vf)[8]) {
      f32x4 s0 = {0.f, 0.f, 0.f, 0.f}, s1 = {0.f, 0.f, 0.f, 0.f};
#pragma unroll
      for (int s = 0; s < 4; ++s) {
        s0 = __builtin_amdgcn_mfma_f32_16x16x32_bf16(__builtin_bit_cast(bf16x8, kf[s]), __builtin_bit_cast(bf16x8, qf[s]), s0, 0, 0, 0);
        s1 = __builtin_amdgcn_mfma_f32_16x16x32_bf16(__builtin_bit_cast(bf16x8, kf[4 + s]), __builtin_bit_cast(bf16x8, qf[s]), s1, 0, 0, 0);
      }
      float t[8];
#pragma unroll
      for (int j = 0; j < 4; ++j) { t[j] = bfr(bfr(s0[j]) * SCALE); t[4 + j] = bfr(bfr(s1[j]) * SCALE); }
      float mx = t[0];
#pragma unroll
      for (int j = 1; j < 8; ++j) mx = fmaxf(mx, t[j]);
      mx = xrow16_max(mx);
      const float mn = fmaxf(m, mx);
      const float alpha = __builtin_amdgcn_exp2f((m - mn) * LOG2E);
      m = mn;
      float p[8], ps = 0.f;
#pragma unroll
      for (int j = 0; j < 8; ++j) { p[j] = __builtin_amdgcn_exp2f((t[j] - mn) * LOG2E); ps += p[j]; }
      l = fmaf(l, alpha, ps);
      const u32x4 pb = {cvtpk(p[0], p[1]), cvtpk(p[2], p[3]), cvtpk(p[4], p[5]), cvtpk(p[6], p[7])};
#pragma unroll
      for (int i = 0; i < 8; ++i) acc[i] *= alpha;
#pragma unroll
      for (int db = 0; db < 8; ++db) {
        const f32x4 z = {0.f, 0.f, 0.f, 0.f};
        const f32x4 d0 = __builtin_amdgcn_mfma_f32_16x16x32_bf16(__builtin_bit_cast(bf16x8, vf[db >> 1]), __builtin_bit_cast(bf16x8, sel[db & 1]), z, 0, 0, 0);
        const f32x4 d1 = __builtin_amdgcn_mfma_f32_16x16x32_bf16(__builtin_bit_cast(bf16x8, vf[4 + (db >> 1)]), __builtin_bit_cast(bf16x8, sel[db & 1]), z, 0, 0, 0);
        const u32x4 vt = {cvtpk(d0[0], d0[1]), cvtpk(d0[2], d0[3]), cvtpk(d1[0], d1[1]), cvtpk(d1[2], d1[3])};
        acc[db] = __builtin_amdgcn_mfma_f32_16x16x32_bf16(__builtin_bit_cast(bf16x8, vt), __builtin_bit_cast(bf16x8, pb), acc[db], 0, 0, 0);
      }
    };
    if (!haveA) LOADKV(kf, vf, cw);
    step(kf, vf);
    if (!haveB) LOADKV(kn, vn, cw + 4);
    step(kn, vn);
#pragma unroll 1
    for (int st = cw + 8; st < 32; st += 4) { LOADKV(kf, vf, st); step(kf, vf); }
    l = xrow16_sum(l);
    if (r < 8) {
      LAS float* rec = MRG + (cw * 8 + r) * PREC;
      if (g == 0) { rec[0] = m; rec[1] = l; }
#pragma unroll
      for (int db = 0; db < 8; ++db) *(LAS f32x4*)(rec + 4 + db * 16 + g * 4) = acc[db];
    }
    cbar();
#pragma unroll
    for (int hh = 0; hh < 2; ++hh) {
      const int h = 2 * cw + hh;
      float M = -INFINITY;
#pragma unroll
      for (int w = 0; w < 4; ++w) M = fmaxf(M, MRG[(w * 8 + h) * PREC]);
      float Ls = 0.f, o0 = 0.f, o1 = 0.f;
#pragma unroll
      for (int w = 0; w < 4; ++w) {
        const LAS float* rec = MRG + (w * 8 + h) * PREC;
        const float f = __builtin_amdgcn_exp2f((rec[0] - M) * LOG2E);
        Ls = fmaf(f, rec[1], Ls); o0 = fmaf(f, rec[4 + 2 * lane], o0); o1 = fmaf(f, rec[5 + 2 * lane], o1);
      }
      gu64* out = (gu64*)(ws + WS_PART + ((size_t)b * 8 + h) * PREC * 4);
      if (lane == 0) st_u64(out, ((u64)__float_as_uint(Ls) << 32) | __float_as_uint(M));
      st_u64(out + 2 + lane, ((u64)__float_as_uint(o1) << 32) | __float_as_uint(o0));
    }
    VM_DRAIN();
    if (lane == 0) st_u64((gu64*)(ws + WS_FLAGC) + 4 * b + cw, 1ull);
  }
  LAS u16* XB0 = (LAS u16*)(L8 + L_XB);
  auto gemv_tile = [&](int q0, const LAS u16* xb) -> float {
    typedef __attribute__((ext_vector_type(4))) short s16x4;
    wait_full(q0); wait_full(q0 + 1);
    f32x4 d0 = {0.f, 0.f, 0.f, 0.f}, d1 = {0.f, 0.f, 0.f, 0.f};
    const int toff = (8 * lg + (lr >> 2)) * 256 + (32 * cw + 4 * (lr & 3)) * 2;
#pragma unroll
    for (int s2 = 0; s2 < 4; ++s2) {
      const u32x4 av = *(const LAS u32x4*)(xb + 32 * s2 + 8 * lg);
      const LAS unsigned char* tb = L8 + RING + ((q0 + (s2 >> 1)) & 7) * SLOT + (s2 & 1) * 8192 + toff;
      const s16x4 p0 = __builtin_amdgcn_ds_read_tr16_b64_v4i16((LAS s16x4*)(tb));
      const s16x4 p1 = __builtin_amdgcn_ds_read_tr16_b64_v4i16((LAS s16x4*)(tb + 1024));
      const s16x4 p2 = __builtin_amdgcn_ds_read_tr16_b64_v4i16((LAS s16x4*)(tb + 32));
      const s16x4 p3 = __builtin_amdgcn_ds_read_tr16_b64_v4i16((LAS s16x4*)(tb + 1056));
      typedef __attribute__((ext_vector_type(8))) short s16x8;
      const s16x8 b0 = __builtin_shufflevector(p0, p1, 0, 1, 2, 3, 4, 5, 6, 7), b1 = __builtin_shufflevector(p2, p3, 0, 1, 2, 3, 4, 5, 6, 7);
      d0 = __builtin_amdgcn_mfma_f32_16x16x32_bf16(__builtin_bit_cast(bf16x8, av), __builtin_bit_cast(bf16x8, b0), d0, 0, 0, 0);
      d1 = __builtin_amdgcn_mfma_f32_16x16x32_bf16(__builtin_bit_cast(bf16x8, av), __builtin_bit_cast(bf16x8, b1), d1, 0, 0, 0);
    }
    return (lane & 16) ? d1[0] : d0[0];
  };
  {
    const float cv = bf2f(cBr);
    if (ct < 128) XB0[ct] = (u16)(__float_as_uint(bfr(cv / (1.f + __expf(-cv)))) >> 16);
    cbar();
    const float r = gemv_tile(32, XB0);
    if (lane < 32) st_gran((gu64*)(ws + WS_GB) + (b >> 5) * 4096 + (b & 31) * 128 + 32 * cw + lane, r);
    cbar();
    if (ct == 0) { FREE[32 & 7] = 33u; FREE[33 & 7] = 34u; }
  }
  {
    const int h = b >> 3, kv = h >> 3;
    const gu64* f = (const gu64*)(ws + WS_FLAGC) + 256 * kv + 4 * lane + ((h & 7) >> 1);
    { u64 t0 = 0; int sp = 0;
      for (;;) { const u64 v = ld_u64(f); if (__all(v == 1ull)) break; if (spin(t0, sp)) break; } }
    asm volatile("" ::: "memory");
    const gu64* pbase = (const gu64*)(ws + WS_PART + ((size_t)kv * 64 * 8 + (h & 7)) * PREC * 4);
    const u64 ml = ld_u64(pbase + (size_t)lane * 4 * PREC);
    u64 ov[16];
#pragma unroll
    for (int i = 0; i < 16; ++i) ov[i] = ld_u64(pbase + (size_t)(16 * lg + i) * 4 * PREC + 2 + 16 * cw + lr);
    const float Ml = __uint_as_float((unsigned)ml), Ll = __uint_as_float((unsigned)(ml >> 32));
    const float M = wave_max(Ml);
    const float f1 = __builtin_amdgcn_exp2f((Ml - M) * LOG2E);
    const float Lt = wave_sum(f1 * Ll);
    const float wgt = f1 / Lt;
    float o0 = 0.f, o1 = 0.f;
#pragma unroll
    for (int i = 0; i < 16; ++i) {
      const float wv = __shfl(wgt, 16 * lg + i);
      o0 = fmaf(wv, __uint_as_float((unsigned)ov[i]), o0); o1 = fmaf(wv, __uint_as_float((unsigned)(ov[i] >> 32)), o1);
    }
    o0 = xrow16_sum(o0); o1 = xrow16_sum(o1);
    if (lane < 16) *(LAS unsigned*)(XB0 + 32 * cw + 2 * lane) = (__float_as_uint(bfr(o0)) >> 16) | (__float_as_uint(bfr(o1)) & 0xffff0000u);
    cbar();
  }
  {
    const float r = gemv_tile(34, XB0);
    if (lane < 32) st_gran((gu64*)(ws + WS_GW) + (b >> 3) * 1024 + (b & 7) * 128 + 32 * cw + lane, r);
  }
  float sbv[2] = {0.f, 0.f}, cbv[2] = {0.f, 0.f}, gav = 0.f, gbv = 0.f;
  if (ffn && cw == 0) {
    const gu64* g = (const gu64*)(ws + WS_GB) + 1024 + 128 * (b / 28) + 2 * lane;
#pragma unroll
    for (int w = 0; w < 2; ++w) {
      float v[16];
      GATHER(16, v, g + (k & 7) * 4096 + (k >> 3) + w * 1024);
      const float r0 = bfr(((v[0] + v[1]) + (v[2] + v[3])) + ((v[4] + v[5]) + (v[6] + v[7])));
      const float r1 = bfr(((v[8] + v[9]) + (v[10] + v[11])) + ((v[12] + v[13]) + (v[14] + v[15])));
      if (w == 0) { sbv[0] = r0; sbv[1] = r1; } else { cbv[0] = r0; cbv[1] = r1; }
    }
  }
  if (b < 64 && cw == 3) {
    const gu64* g = (const gu64*)(ws + WS_GB) + 16 * b + (lane & 15);
    float v[16];
    GATHER(16, v, g + (k & 7) * 4096 + (k >> 3) * 3072);
    gav = bfr(((v[0] + v[1]) + (v[2] + v[3])) + ((v[4] + v[5]) + (v[6] + v[7])));
    gbv = bfr(((v[8] + v[9]) + (v[10] + v[11])) + ((v[12] + v[13]) + (v[14] + v[15])));
  }
  float x1r = 0.f;
  if (b < 64 && cw == 3) {
    const gu64* g = (const gu64*)(ws + WS_GW) + (size_t)(lane >> 4) * 1024 + 16 * b + (lane & 15);
    float v[8];
    GATHER(8, v, g + k * 4096);
    float s = ((v[0] + v[1]) + (v[2] + v[3])) + ((v[4] + v[5]) + (v[6] + v[7]));
    s = xrow16_sum(s);
    x1r = bfr(bf2f(a.x[16 * b + (lane & 15)]) + bfr(gav * bfr(s)));
    { const float nb = __shfl_down(x1r, 1);
      if (lane < 16 && !(lane & 1)) st_u64((gu64*)(ws + WS_GX) + 8 * b + (lane >> 1), (1ull << 32) | (u64)((__float_as_uint(x1r) >> 16) | (__float_as_uint(nb) & 0xffff0000u))); }
  }
  if (ffn) {
    cbar();
    if (cw == 0) {
      const gu64* g = (const gu64*)(ws + WS_GX) + lane;
      float v[8];
      GATHER(8, v, g + k * 64);
      float xv[16];
#pragma unroll
      for (int i = 0; i < 8; ++i) { const unsigned u = __float_as_uint(v[i]); xv[2 * i] = lo16(u); xv[2 * i + 1] = hi16(u); }
      float s = 0.f;
#pragma unroll
      for (int i = 0; i < 16; ++i) s += xv[i];
      const float mean = wave_sum(s) * (1.f / 1024.f);
      float qv = 0.f;
#pragma unroll
      for (int i = 0; i < 16; ++i) { const float d = xv[i] - mean; qv = fmaf(d, d, qv); }
      const float var = wave_sum(qv) * (1.f / 1024.f);
      const float mb = bfr(mean), sd = bfr(sqrtf(bfr(bfr(var) + 1e-5f)));
      const int eks = b / 28;
      float x0 = xv[0], x1 = xv[1];
#pragma unroll
      for (int i = 1; i < 8; ++i) { x0 = (eks == i) ? xv[2 * i] : x0; x1 = (eks == i) ? xv[2 * i + 1] : x1; }
      const float h0 = bfr(bfr(bfr(bfr(x0 - mb) / sd) * bfr(1.f + cbv[0])) + sbv[0]);
      const float h1 = bfr(bfr(bfr(bfr(x1 - mb) / sd) * bfr(1.f + cbv[1])) + sbv[1]);
      *(LAS unsigned*)(XB0 + 2 * lane) = (__float_as_uint(h0) >> 16) | (__float_as_uint(h1) & 0xffff0000u);
    }
    cbar();
    const float rg = gemv_tile(36, XB0);
    const float ru = gemv_tile(38, XB0);
    if (lane < 32) {
      st_gran((gu64*)(ws + WS_GG) + (b / 28) * FF + (b % 28) * 128 + 32 * cw + lane, rg);
      st_gran((gu64*)(ws + WS_GU) + (b / 28) * FF + (b % 28) * 128 + 32 * cw + lane, ru);
    }
  }
  {
    cbar();
    if (cw < 2) {
      const int c = ct < 112 ? ct : 0;
      const gu64* g = (const gu64*)(ws + WS_GG) + 112 * (b >> 3) + c;
      float v[16];
      GATHER(16, v, g + (k & 7) * FF + (k >> 3) * (8 * FF));
      const float gt = bfr(((v[0] + v[1]) + (v[2] + v[3])) + ((v[4] + v[5]) + (v[6] + v[7])));
      const float up = bfr(((v[8] + v[9]) + (v[10] + v[11])) + ((v[12] + v[13]) + (v[14] + v[15])));
      const float inner = 0.7978845608028654f * (gt + 0.044715f * gt * gt * gt);
      const float ge = bfr(0.5f * gt * (1.f + tanhf(inner)));
      XB0[ct] = ct < 112 ? (u16)(__float_as_uint(bfr(ge * up)) >> 16) : (u16)0;
    }
    cbar();
  }
  {
    const float r = gemv_tile(QD, XB0);
    if (lane < 32) st_gran((gu64*)(ws + WS_GD) + (b >> 3) * 1024 + (b & 7) * 128 + 32 * cw + lane, r);
  }
  if (b < 64 && cw == 3) {
    const gu64* g = (const gu64*)(ws + WS_GD) + (size_t)(lane >> 4) * 1024 + 16 * b + (lane & 15);
    float v[8];
    GATHER(8, v, g + k * 4096);
    float s = ((v[0] + v[1]) + (v[2] + v[3])) + ((v[4] + v[5]) + (v[6] + v[7]));
    s = xrow16_sum(s);
    const float o = x1r + bfr(gbv * bfr(s));
    if (lane < 16) a.out[16 * b + lane] = (u16)(__float_as_uint(bfr(o)) >> 16);
  }
}

extern "C" void kernel_launch(void* const* d_in, const int* in_sizes, int n_in, void* d_out, int out_size, void* d_ws, size_t ws_size,
                              hipStream_t stream) {
  Args a;
  a.x = (const u16*)d_in[0]; a.kc = (const u16*)d_in[1]; a.vc = (const u16*)d_in[2]; a.wq = (const u16*)d_in[3]; a.wo = (const u16*)d_in[4];
  a.wg = (const u16*)d_in[6]; a.wu = (const u16*)d_in[7]; a.wd = (const u16*)d_in[8]; a.c = (const u16*)d_in[9]; a.wmod = (const u16*)d_in[10];
  a.ws = (unsigned char*)d_ws; a.out = (u16*)d_out;
  fused_kernel<<<NWG, NTHR, 0, stream>>>(a);
}
```
